```python
import torch

def _softmax(x, axis=-1):
    return torch.softmax(x, dim=axis)
def _gelu(x):
    return torch.nn.functional.gelu(x, approximate="tanh")
def _silu(x):
    return torch.nn.functional.silu(x)
def _sigmoid(x):
    return torch.sigmoid(x)
def _layernorm(x, axis=-1, eps=1e-5):
    m = x.mean(dim=axis, keepdim=True)
    v = x.var(dim=axis, keepdim=True, unbiased=False)
    return (x - m) / torch.sqrt(v + eps)
def _scatter_add(tgt, idx, src, axis=0):
    return tgt.clone().index_add_(axis, idx, src)
def _segment_sum(src, seg, n):
    out = torch.zeros((n,) + src.shape[1:], dtype=src.dtype)
    return out.index_add_(0, seg, src)
def _pad(x, pw):
    flat = tuple(v for pair in reversed(pw) for v in pair)
    return torch.nn.functional.pad(x, flat)

kernel_name = 'gated_mix__mha_block_cached__dense_activation_silu__decode'

def setup_inputs():

    _w = (lambda a: (lambda t: t.to(torch.bfloat16) if t.is_floating_point() else t)(torch.as_tensor(a)))
    def _A_setup():
        B, Sq, D = 2, 1, 640
        H, Sk, Dh = 32, 4096, 128
        return {
            "x": _w((lambda *s: torch.randn(*s, dtype=torch.bfloat16) * 0.05)(B, Sq, D)),
            "k_cache": _w((lambda *s: torch.randn(*s, dtype=torch.bfloat16) * 6.4)(B, H, Sk, Dh)),
            "v_cache": _w((lambda *s: torch.randn(*s, dtype=torch.bfloat16) * 0.2)(B, H, Sk, Dh)),
            "wq": _w((lambda *s: torch.randn(*s, dtype=torch.bfloat16) * 6.4)(D, H * Dh) * (D ** -0.5)),
            "wo": _w((lambda *s: torch.randn(*s, dtype=torch.bfloat16) * 0.2)(H * Dh, D) * ((H * Dh) ** -0.5)),
        }
    _A_in = _A_setup()

    def _B_setup():
        B, S, D, F = 2, 1, 640, 1728
        return {
            "x": _w((lambda *s: torch.randn(*s, dtype=torch.bfloat16) * 0.05)(B, S, D)),
            "w1": _w((lambda *s: torch.randn(*s, dtype=torch.bfloat16) * 6.4)(D, F) * (D ** -0.5)),
            "w2": _w((lambda *s: torch.randn(*s, dtype=torch.bfloat16) * 0.05)(F, D) * (F ** -0.5)),
        }
    _B_in = _B_setup()

    return {
        "A_x": _A_in["x"],
        "A_k_cache": _A_in["k_cache"],
        "A_v_cache": _A_in["v_cache"],
        "A_wq": _A_in["wq"],
        "A_wo": _A_in["wo"],
        "B_x": _B_in["x"],
        "B_w1": _B_in["w1"],
        "B_w2": _B_in["w2"],
        "gate_w": _w((lambda *s: torch.randn(*s, dtype=torch.bfloat16) * 0.05)(640) * 16.0),
    }

def reference(A_x, A_k_cache, A_v_cache, A_wq, A_wo, B_x, B_w1, B_w2, gate_w):
    def _A_ref(x, k_cache, v_cache, wq, wo):
        B, Sq, D = x.shape
        H, Dh = 32, 128
        q = (lambda x, *d: x.permute(*d))((x @ wq).reshape(B, Sq, H, Dh), 0, 2, 1, 3)
        scores = torch.einsum("bhqd,bhkd->bhqk", q, k_cache) * (Dh ** -0.5)
        p = _softmax(scores, axis=-1)
        o = torch.einsum("bhqk,bhkd->bhqd", p, v_cache)
        return (lambda x, *d: x.permute(*d))(o, 0, 2, 1, 3).reshape(B, Sq, H * Dh) @ wo

    def _B_ref(x, w1, w2):
        h = torch.einsum("bsd,df->bsf", x, w1)
        h = _silu(h)
        out = torch.einsum("bsf,fd->bsd", h, w2)
        return out

    x = A_x
    g = _sigmoid((x * gate_w).sum(-1, keepdims=True))
    a = _A_ref(x, A_k_cache, A_v_cache, A_wq, A_wo)
    b = _B_ref(x, B_w1, B_w2)
    return g * a + (1 - g) * b
```

```cpp
#include <hip/hip_runtime.h>
#include <hip/hip_bf16.h>
#include <stdint.h>

typedef unsigned int u32;
typedef unsigned short u16;
typedef __attribute__((ext_vector_type(4))) unsigned u32x4;
typedef __attribute__((ext_vector_type(2))) unsigned u32x2;
typedef __attribute__((ext_vector_type(4))) float f32x4;
typedef __attribute__((ext_vector_type(2))) float f32x2;
typedef __attribute__((ext_vector_type(2))) __bf16 bf16x2;

namespace {

constexpr int NB = 2, DM = 640, NH = 32, SK = 4096, DH = 128, FF = 1728;
constexpr int NSPLIT = 4, TOKWG = SK / NSPLIT, NWAVE = 8, TOKW = TOKWG / NWAVE;
constexpr int NFG = FF / 8;
constexpr float LOG2E = 1.4426950408889634f;
constexpr float SCALE = 0.088388347648318440f;

constexpr size_t WS_P = 0;
constexpr size_t WS_ML = WS_P + (size_t)NB * NH * NSPLIT * DM;
constexpr size_t WS_F = WS_ML + (size_t)NB * NH * NSPLIT * 2;

__device__ __forceinline__ float dot2(u32 a, u32 b, float acc) {
  return __builtin_amdgcn_fdot2_f32_bf16(__builtin_bit_cast(bf16x2, a), __builtin_bit_cast(bf16x2, b), acc, false);
}
__device__ __forceinline__ float bflo(u32 v) { return __builtin_bit_cast(float, v << 16); }
__device__ __forceinline__ float bfhi(u32 v) { return __builtin_bit_cast(float, v & 0xffff0000u); }
__device__ __forceinline__ float rbf(float x) { return __bfloat162float(__float2bfloat16(x)); }
__device__ __forceinline__ u32 pk_bf(float lo, float hi) {
  const u32 a = __builtin_bit_cast(u16, __float2bfloat16(lo));
  const u32 b = __builtin_bit_cast(u16, __float2bfloat16(hi));
  return a | (b << 16);
}
template <int CTRL> __device__ __forceinline__ float dpp(float x) {
  return __builtin_bit_cast(float, __builtin_amdgcn_mov_dpp(__builtin_bit_cast(int, x), CTRL, 0xf, 0xf, true));
}
constexpr int XOR1 = 0xB1, XOR2 = 0x4E, XOR7 = 0x141, XOR8 = 0x128, BCAST = 0x150;

__device__ __forceinline__ float xrow_sum(float x) {
  auto s = __builtin_amdgcn_permlane16_swap(__float_as_uint(x), __float_as_uint(x), false, false);
  x = __uint_as_float(s[0]) + __uint_as_float(s[1]);
  auto t = __builtin_amdgcn_permlane32_swap(__float_as_uint(x), __float_as_uint(x), false, false);
  return __uint_as_float(t[0]) + __uint_as_float(t[1]);
}
__device__ __forceinline__ float xrow_max(float x) {
  auto s = __builtin_amdgcn_permlane16_swap(__float_as_uint(x), __float_as_uint(x), false, false);
  x = fmaxf(__uint_as_float(s[0]), __uint_as_float(s[1]));
  auto t = __builtin_amdgcn_permlane32_swap(__float_as_uint(x), __float_as_uint(x), false, false);
  return fmaxf(__uint_as_float(t[0]), __uint_as_float(t[1]));
}
__device__ __forceinline__ float row_sum(float x) {
  x += dpp<XOR1>(x); x += dpp<XOR2>(x); x += dpp<XOR7>(x); x += dpp<XOR8>(x); return x;
}
__device__ __forceinline__ float row_max(float x) {
  x = fmaxf(x, dpp<XOR1>(x)); x = fmaxf(x, dpp<XOR2>(x)); x = fmaxf(x, dpp<XOR7>(x)); x = fmaxf(x, dpp<XOR8>(x)); return x;
}
__device__ __forceinline__ float tr_reduce16(float (&x)[16], int c) {
#define TR_STEP(HALF, CTRL, BIT)                                                     \
  _Pragma("unroll") for (int h = 0; h < (HALF); ++h) {                               \
    const float lo_ = x[h], hi_ = x[h + (HALF)];                                     \
    const bool up_ = (c & (BIT)) != 0;                                               \
    const float send_ = up_ ? lo_ : hi_, keep_ = up_ ? hi_ : lo_;                    \
    x[h] = keep_ + dpp<CTRL>(send_);                                                 \
  }
  TR_STEP(8, XOR8, 8) TR_STEP(4, XOR7, 4) TR_STEP(2, XOR2, 2) TR_STEP(1, XOR1, 1)
#undef TR_STEP
  return x[0];
}

#define SCHED_FENCE() __builtin_amdgcn_sched_barrier(0)

__device__ __forceinline__ u32x4 ld_nt(const u16* ub, u32 off) {
  return __builtin_nontemporal_load(reinterpret_cast<const u32x4*>(reinterpret_cast<const char*>(ub) + off));
}
__device__ __forceinline__ u32x4 ld16(const u16* ub, u32 off) {
  return *reinterpret_cast<const u32x4*>(reinterpret_cast<const char*>(ub) + off);
}
__device__ __forceinline__ u32x2 ld8(const u16* ub, u32 off) {
  return *reinterpret_cast<const u32x2*>(reinterpret_cast<const char*>(ub) + off);
}
__device__ __forceinline__ u32 ld2(const u16* ub, u32 off) {
  return *reinterpret_cast<const u16*>(reinterpret_cast<const char*>(ub) + off);
}

constexpr int L_QP = 0;
constexpr int L_F1 = L_QP + NWAVE * DH;
constexpr int L_OP = L_F1 + NWAVE * 16;
constexpr int L_ML = L_OP + NWAVE * DH;
constexpr int L_RED = L_ML + NWAVE * 2;
constexpr int L_END = L_RED + NWAVE * DM;

__global__ void __launch_bounds__(512, 2)
k_main(const u16* __restrict__ x, const u16* __restrict__ kc, const u16* __restrict__ vc, const u16* __restrict__ wq,
       const u16* __restrict__ wo, const u16* __restrict__ w1, const u16* __restrict__ w2, float* __restrict__ ws) {
  __shared__ __attribute__((aligned(16))) float lds[L_END];
  const int bid = blockIdx.x, xcd = bid & 7, idx = bid >> 3;
  const int h = xcd * 4 + (idx >> 3), b = (idx >> 2) & 1, s = idx & 3;
  const int tid = threadIdx.x, lane = tid & 63, wave = __builtin_amdgcn_readfirstlane(tid >> 6);
  const int r = lane >> 4, c = lane & 15;
  const bool ffn_on = idx < 27;
  const int fg = xcd * 27 + (ffn_on ? idx : 26);

  u32x2 xq[5];
#pragma unroll
  for (int j = 0; j < 5; ++j) xq[j] = ld8(x + b * DM + wave * 80 + 4 * j, r * 40);
#define KS(i) ((i) < 24 ? 20 + (i) : (i) - 24)
#define VS(i) ((i) < 28 ? 8 + (i) : 8 + (i))
#define WS(i) ((i) < 4 ? 40 + (i) : (i) - 4)
  u32x4 R[44];
  {
    const u32 off = (u32)(r * 20) * (NH * DH * 2) + 16 * c;
#pragma unroll
    for (int j = 0; j < 20; ++j) R[j] = ld16(wq + (size_t)(wave * 80 + j) * (NH * DH) + h * DH, off);
  }
  const u32x4 w1a = ld16(w1 + (size_t)(wave * 80) * FF + fg * 8, (u32)lane * (FF * 2));
  const u32x4 w1b = ld16(w1 + (size_t)(wave * 80 + 64) * FF + fg * 8, (u32)c * (FF * 2));
  const u32 xa0 = ld2(x + wave * 80, 2 * lane), xa1 = ld2(x + DM + wave * 80, 2 * lane);
  const u32 xb0 = ld2(x + wave * 80 + 64, 2 * c), xb1 = ld2(x + DM + wave * 80 + 64, 2 * c);
  SCHED_FENCE();
  const size_t kvbase = ((size_t)(b * NH + h) * SK + s * TOKWG + wave * TOKW) * DH;
  const u16* kp = kc + kvbase;
  const u16* vp = vc + kvbase;
  const u32 loff = lane * 16;
#pragma unroll
  for (int i = 0; i < 24; ++i) { R[KS(i)] = ld_nt(kp + i * 4 * DH, loff); SCHED_FENCE(); }

  {
    float qa[8];
#pragma unroll
    for (int k = 0; k < 8; ++k) qa[k] = 0.f;
#pragma unroll
    for (int j = 0; j < 20; ++j) {
      const u32 xw = xq[j >> 2][(j >> 1) & 1];
      const float xv = (j & 1) ? bfhi(xw) : bflo(xw);
#pragma unroll
      for (int k = 0; k < 4; ++k) {
        qa[2 * k] = fmaf(xv, bflo(R[j][k]), qa[2 * k]);
        qa[2 * k + 1] = fmaf(xv, bfhi(R[j][k]), qa[2 * k + 1]);
      }
    }
#pragma unroll
    for (int k = 0; k < 8; ++k) qa[k] = xrow_sum(qa[k]);
    if (r == 0) {
      *reinterpret_cast<f32x4*>(&lds[L_QP + wave * DH + 8 * c]) = f32x4{qa[0], qa[1], qa[2], qa[3]};
      *reinterpret_cast<f32x4*>(&lds[L_QP + wave * DH + 8 * c + 4]) = f32x4{qa[4], qa[5], qa[6], qa[7]};
    }
    float fa[16];
    const float a0 = bflo(xa0), a1 = bflo(xa1);
    const float b0 = lane < 16 ? bflo(xb0) : 0.f, b1 = lane < 16 ? bflo(xb1) : 0.f;
#pragma unroll
    for (int k = 0; k < 4; ++k) {
      fa[2 * k] = fmaf(a0, bflo(w1a[k]), b0 * bflo(w1b[k]));
      fa[2 * k + 1] = fmaf(a0, bfhi(w1a[k]), b0 * bfhi(w1b[k]));
      fa[8 + 2 * k] = fmaf(a1, bflo(w1a[k]), b1 * bflo(w1b[k]));
      fa[8 + 2 * k + 1] = fmaf(a1, bfhi(w1a[k]), b1 * bfhi(w1b[k]));
    }
    float ft = tr_reduce16(fa, c);
    ft = xrow_sum(ft);
    if (r == 0) lds[L_F1 + wave * 16 + c] = ft;
  }
  SCHED_FENCE();
  u32x2 w2r[8];
  {
    const u32 col = (tid < 160 ? tid : 0) * 8;
#pragma unroll
    for (int j = 0; j < 8; ++j) w2r[j] = ld8(w2 + (size_t)(fg * 8 + j) * DM, col);
  }
  SCHED_FENCE();
#pragma unroll
  for (int i = 24; i < 32; ++i) { R[KS(i)] = ld_nt(kp + i * 4 * DH, loff); SCHED_FENCE(); }
#pragma unroll
  for (int i = 0; i < 12; ++i) { R[VS(i)] = ld_nt(vp + i * 4 * DH, loff); SCHED_FENCE(); }
  __syncthreads();
  u32 qpk[4];
  {
    f32x4 q0 = {0.f, 0.f, 0.f, 0.f}, q1 = {0.f, 0.f, 0.f, 0.f};
#pragma unroll
    for (int w = 0; w < NWAVE; ++w) {
      q0 += *reinterpret_cast<const f32x4*>(&lds[L_QP + w * DH + 8 * c]);
      q1 += *reinterpret_cast<const f32x4*>(&lds[L_QP + w * DH + 8 * c + 4]);
    }
    qpk[0] = pk_bf(q0[0], q0[1]); qpk[1] = pk_bf(q0[2], q0[3]);
    qpk[2] = pk_bf(q1[0], q1[1]); qpk[3] = pk_bf(q1[2], q1[3]);
  }
  float hval;
  {
    float t = 0.f;
#pragma unroll
    for (int w = 0; w < NWAVE; ++w) t += lds[L_F1 + w * 16 + c];
    t = rbf(t);
    hval = rbf(t / (1.f + __expf(-t)));
  }

  float sc[2];
  const u16* wop = wo + (size_t)(h * DH + wave * 16) * DM;
  const u32 wooff = (u32)r * (4 * DM * 2) + 16 * c;
#pragma unroll
  for (int g = 0; g < 2; ++g) {
    float xx[16];
#pragma unroll
    for (int i = 0; i < 16; ++i) {
      const int kidx = g * 16 + i;
      const u32x4 kk = R[KS(kidx)];
      float d = dot2(kk[0], qpk[0], 0.f);
      d = dot2(kk[1], qpk[1], d);
      d = dot2(kk[2], qpk[2], d);
      xx[i] = dot2(kk[3], qpk[3], d);
    }
    SCHED_FENCE();
    if (g == 0) {
#pragma unroll
      for (int i = 12; i < 28; ++i) { R[VS(i)] = ld_nt(vp + i * 4 * DH, loff); SCHED_FENCE(); }
    } else {
#pragma unroll
      for (int i = 28; i < 32; ++i) { R[VS(i)] = ld_nt(vp + i * 4 * DH, loff); SCHED_FENCE(); }
#pragma unroll
      for (int i = 0; i < 12; ++i) R[WS(i)] = ld16(wop + (i / 5) * DM + (i % 5) * 128, wooff);
    }
    SCHED_FENCE();
    const float t = tr_reduce16(xx, c);
    sc[g] = rbf(rbf(t) * SCALE);
  }
  float m = xrow_max(row_max(fmaxf(sc[0], sc[1])));
  const float m2 = m * LOG2E;
  float p[2];
  p[0] = __builtin_amdgcn_exp2f(fmaf(sc[0], LOG2E, -m2));
  p[1] = __builtin_amdgcn_exp2f(fmaf(sc[1], LOG2E, -m2));
  const float lsum = xrow_sum(row_sum(p[0] + p[1]));

  {
    float f0[4] = {0.f, 0.f, 0.f, 0.f}, f1[4] = {0.f, 0.f, 0.f, 0.f};
#define FFN2_STEP(J)                                                                  \
    {                                                                                 \
      const float h0 = dpp<BCAST + (J)>(hval), h1 = dpp<BCAST + 8 + (J)>(hval);       \
      const float w0 = bflo(w2r[J][0]), w1_ = bfhi(w2r[J][0]), w2_ = bflo(w2r[J][1]), w3 = bfhi(w2r[J][1]); \
      f0[0] = fmaf(h0, w0, f0[0]); f0[1] = fmaf(h0, w1_, f0[1]); f0[2] = fmaf(h0, w2_, f0[2]); f0[3] = fmaf(h0, w3, f0[3]); \
      f1[0] = fmaf(h1, w0, f1[0]); f1[1] = fmaf(h1, w1_, f1[1]); f1[2] = fmaf(h1, w2_, f1[2]); f1[3] = fmaf(h1, w3, f1[3]); \
    }
    FFN2_STEP(0) FFN2_STEP(1) FFN2_STEP(2) FFN2_STEP(3) FFN2_STEP(4) FFN2_STEP(5) FFN2_STEP(6) FFN2_STEP(7)
#undef FFN2_STEP
    asm volatile("" : "+v"(f0[0]), "+v"(f0[1]), "+v"(f0[2]), "+v"(f0[3]), "+v"(f1[0]), "+v"(f1[1]), "+v"(f1[2]), "+v"(f1[3]));
    if (ffn_on && tid < 160) {
      float* fp = ws + WS_F + (size_t)fg * NB * DM + tid * 4;
      *reinterpret_cast<f32x4*>(fp) = f32x4{f0[0], f0[1], f0[2], f0[3]};
      *reinterpret_cast<f32x4*>(fp + DM) = f32x4{f1[0], f1[1], f1[2], f1[3]};
    }
  }
  SCHED_FENCE();

  float acc[8];
#pragma unroll
  for (int k = 0; k < 8; ++k) acc[k] = 0.f;
#define PV_STEP(I)                                                                    \
  {                                                                                   \
    const float pb = dpp<BCAST + ((I) & 15)>(p[(I) >> 4]);                            \
    const u32x4 vv = R[VS(I)];                                                        \
    _Pragma("unroll") for (int k = 0; k < 4; ++k) {                                   \
      acc[2 * k] = fmaf(pb, bflo(vv[k]), acc[2 * k]);                                 \
      acc[2 * k + 1] = fmaf(pb, bfhi(vv[k]), acc[2 * k + 1]);                         \
    }                                                                                 \
  }
  PV_STEP(0) PV_STEP(1) PV_STEP(2) PV_STEP(3) PV_STEP(4) PV_STEP(5) PV_STEP(6) PV_STEP(7)
  SCHED_FENCE();
#pragma unroll
  for (int i = 12; i < 20; ++i) R[WS(i)] = ld16(wop + (i / 5) * DM + (i % 5) * 128, wooff);
  SCHED_FENCE();
  PV_STEP(8) PV_STEP(9) PV_STEP(10) PV_STEP(11) PV_STEP(12) PV_STEP(13) PV_STEP(14) PV_STEP(15)
  PV_STEP(16) PV_STEP(17) PV_STEP(18) PV_STEP(19) PV_STEP(20) PV_STEP(21) PV_STEP(22) PV_STEP(23)
  PV_STEP(24) PV_STEP(25) PV_STEP(26) PV_STEP(27) PV_STEP(28) PV_STEP(29) PV_STEP(30) PV_STEP(31)
#undef PV_STEP
#pragma unroll
  for (int k = 0; k < 8; ++k) acc[k] = xrow_sum(acc[k]);
  if (r == 0) {
    *reinterpret_cast<f32x4*>(&lds[L_OP + wave * DH + 8 * c]) = f32x4{acc[0], acc[1], acc[2], acc[3]};
    *reinterpret_cast<f32x4*>(&lds[L_OP + wave * DH + 8 * c + 4]) = f32x4{acc[4], acc[5], acc[6], acc[7]};
    if (c == 0) *reinterpret_cast<f32x2*>(&lds[L_ML + wave * 2]) = f32x2{m2, lsum};
  }
  __syncthreads();

  {
    float mw[8], lw[8];
#pragma unroll
    for (int w = 0; w < 4; ++w) {
      const f32x4 t = *reinterpret_cast<const f32x4*>(&lds[L_ML + 4 * w]);
      mw[2 * w] = t[0]; lw[2 * w] = t[1]; mw[2 * w + 1] = t[2]; lw[2 * w + 1] = t[3];
    }
    float M = mw[0];
#pragma unroll
    for (int w = 1; w < 8; ++w) M = fmaxf(M, mw[w]);
    f32x4 o4 = {0.f, 0.f, 0.f, 0.f};
    float L = 0.f;
#pragma unroll
    for (int w = 0; w < 8; ++w) {
      const float e = __builtin_amdgcn_exp2f(mw[w] - M);
      L = fmaf(e, lw[w], L);
      o4 += e * *reinterpret_cast<const f32x4*>(&lds[L_OP + w * DH + (wave * 4 + r) * 4]);
    }
    if (tid == 0) *reinterpret_cast<f32x2*>(ws + WS_ML + (size_t)((b * NH + h) * NSPLIT + s) * 2) = f32x2{M, L};
    float y[40];
#pragma unroll
    for (int k = 0; k < 40; ++k) y[k] = 0.f;
#pragma unroll
    for (int i = 0; i < 20; ++i) {
      const float ov = o4[i / 5];
      const int seg = i % 5;
      const u32x4 ww = R[WS(i)];
#pragma unroll
      for (int k = 0; k < 4; ++k) {
        y[(2 * k) * 5 + seg] = fmaf(ov, bflo(ww[k]), y[(2 * k) * 5 + seg]);
        y[(2 * k + 1) * 5 + seg] = fmaf(ov, bfhi(ww[k]), y[(2 * k + 1) * 5 + seg]);
      }
    }
    float z[20];
#pragma unroll
    for (int v = 0; v < 20; ++v) {
      auto t = __builtin_amdgcn_permlane32_swap(__float_as_uint(y[v]), __float_as_uint(y[v + 20]), false, false);
      z[v] = __uint_as_float(t[0]) + __uint_as_float(t[1]);
    }
    float u[10];
#pragma unroll
    for (int v = 0; v < 10; ++v) {
      auto t = __builtin_amdgcn_permlane16_swap(__float_as_uint(z[v]), __float_as_uint(z[v + 10]), false, false);
      u[v] = __uint_as_float(t[0]) + __uint_as_float(t[1]);
    }
#pragma unroll
    for (int seg = 0; seg < 5; ++seg)
      *reinterpret_cast<f32x2*>(&lds[L_RED + wave * DM + seg * 128 + 8 * c + 2 * r]) = f32x2{u[seg], u[5 + seg]};
  }
  __syncthreads();
  if (tid < 160) {
    f32x4 t = {0.f, 0.f, 0.f, 0.f};
#pragma unroll
    for (int w = 0; w < NWAVE; ++w) t += *reinterpret_cast<const f32x4*>(&lds[L_RED + w * DM + tid * 4]);
    *reinterpret_cast<f32x4*>(ws + WS_P + (size_t)((b * NH + h) * NSPLIT + s) * DM + tid * 4) = t;
  }
}

__global__ void __launch_bounds__(256)
k_fin(const u16* __restrict__ x, const u16* __restrict__ gw, const float* __restrict__ ws, u16* __restrict__ out) {
  __shared__ __attribute__((aligned(16))) float red[4][2][32];
  const int b = blockIdx.x / 20, cb = blockIdx.x % 20;
  const int tid = threadIdx.x, lane = tid & 63, wave = tid >> 6;
  const int c4 = tid & 7, rg = tid >> 3;
  const int col = cb * 32 + c4 * 4;
  f32x4 pr[4];
#pragma unroll
  for (int s = 0; s < 4; ++s)
    pr[s] = *reinterpret_cast<const f32x4*>(ws + WS_P + (size_t)((b * NH + rg) * NSPLIT + s) * DM + col);
  const f32x4 ml0 = *reinterpret_cast<const f32x4*>(ws + WS_ML + (size_t)(b * NH + rg) * NSPLIT * 2);
  const f32x4 ml1 = *reinterpret_cast<const f32x4*>(ws + WS_ML + (size_t)(b * NH + rg) * NSPLIT * 2 + 4);
  f32x4 fr[7];
#pragma unroll
  for (int j = 0; j < 7; ++j) {
    const int f = rg + 32 * j;
    const int fc = f < NFG ? f : NFG - 1;
    fr[j] = *reinterpret_cast<const f32x4*>(ws + WS_F + (size_t)(fc * NB + b) * DM + col);
  }
  u32 xg[5], gg[5];
#pragma unroll
  for (int j = 0; j < 5; ++j) {
    xg[j] = *reinterpret_cast<const u32*>(x + b * DM + 2 * (lane + 64 * j));
    gg[j] = *reinterpret_cast<const u32*>(gw + 2 * (lane + 64 * j));
  }
  float gs = 0.f;
#pragma unroll
  for (int j = 0; j < 5; ++j) gs += rbf(bflo(xg[j]) * bflo(gg[j])) + rbf(bfhi(xg[j]) * bfhi(gg[j]));
  gs = xrow_sum(row_sum(gs));
  gs = rbf(gs);
  const float g = rbf(1.f / (1.f + __expf(-gs)));
  const float M = fmaxf(fmaxf(ml0[0], ml0[2]), fmaxf(ml1[0], ml1[2]));
  const float e0 = __builtin_amdgcn_exp2f(ml0[0] - M), e1 = __builtin_amdgcn_exp2f(ml0[2] - M);
  const float e2 = __builtin_amdgcn_exp2f(ml1[0] - M), e3 = __builtin_amdgcn_exp2f(ml1[2] - M);
  const float L = e0 * ml0[1] + e1 * ml0[3] + e2 * ml1[1] + e3 * ml1[3];
  const float inv = 1.f / L;
  f32x4 a = (e0 * inv) * pr[0] + (e1 * inv) * pr[1] + (e2 * inv) * pr[2] + (e3 * inv) * pr[3];
  f32x4 f = fr[0] + fr[1] + fr[2] + fr[3] + fr[4] + fr[5];
  if (rg + 32 * 6 < NFG) f += fr[6];
#pragma unroll
  for (int k = 0; k < 4; ++k) {
    a[k] += dpp<XOR8>(a[k]); a[k] = xrow_sum(a[k]);
    f[k] += dpp<XOR8>(f[k]); f[k] = xrow_sum(f[k]);
  }
  if (lane < 8) {
    *reinterpret_cast<f32x4*>(&red[wave][0][c4 * 4]) = a;
    *reinterpret_cast<f32x4*>(&red[wave][1][c4 * 4]) = f;
  }
  __syncthreads();
  if (tid < 32) {
    const float av = rbf(red[0][0][tid] + red[1][0][tid] + red[2][0][tid] + red[3][0][tid]);
    const float fv = rbf(red[0][1][tid] + red[1][1][tid] + red[2][1][tid] + red[3][1][tid]);
    const float t1 = rbf(g * av), t2 = rbf(rbf(1.f - g) * fv);
    out[b * DM + cb * 32 + tid] = __builtin_bit_cast(u16, __float2bfloat16(t1 + t2));
  }
}

}

extern "C" void kernel_launch(void* const* d_in, const int* in_sizes, int n_in, void* d_out, int out_size, void* d_ws,
                              size_t ws_size, hipStream_t stream) {
  const u16* x = (const u16*)d_in[0];
  const u16* kc = (const u16*)d_in[1];
  const u16* vc = (const u16*)d_in[2];
  const u16* wq = (const u16*)d_in[3];
  const u16* wo = (const u16*)d_in[4];
  const u16* w1 = (const u16*)d_in[6];
  const u16* w2 = (const u16*)d_in[7];
  const u16* gw = (const u16*)d_in[8];
  float* ws = (float*)d_ws;
  k_main<<<dim3(NB * NH * NSPLIT), dim3(512), 0, stream>>>(x, kc, vc, wq, wo, w1, w2, ws);
  k_fin<<<dim3(NB * 20), dim3(256), 0, stream>>>(x, gw, ws, (u16*)d_out);
}
```

```cpp
#define W_NT 0
#include <hip/hip_runtime.h>
#include <hip/hip_bf16.h>
#include <stdint.h>

typedef unsigned int u32;
typedef unsigned short u16;
typedef __attribute__((ext_vector_type(4))) unsigned u32x4;
typedef __attribute__((ext_vector_type(2))) unsigned u32x2;
typedef __attribute__((ext_vector_type(4))) float f32x4;
typedef __attribute__((ext_vector_type(2))) float f32x2;
typedef __attribute__((ext_vector_type(2))) __bf16 bf16x2;

namespace {

constexpr int NB = 2, DM = 640, NH = 32, SK = 4096, DH = 128, FF = 1728;
constexpr int NSPLIT = 4, TOKWG = SK / NSPLIT, NWAVE = 8, TOKW = TOKWG / NWAVE;
constexpr int NFG = FF / 8;
constexpr float LOG2E = 1.4426950408889634f;
constexpr float SCALE = 0.088388347648318440f;

constexpr size_t WS_P = 0;
constexpr size_t WS_ML = WS_P + (size_t)NB * NH * NSPLIT * DM;
constexpr size_t WS_F = WS_ML + (size_t)NB * NH * NSPLIT * 2;

__device__ __forceinline__ float dot2(u32 a, u32 b, float acc) {
  return __builtin_amdgcn_fdot2_f32_bf16(__builtin_bit_cast(bf16x2, a), __builtin_bit_cast(bf16x2, b), acc, false);
}
__device__ __forceinline__ float bflo(u32 v) { return __builtin_bit_cast(float, v << 16); }
__device__ __forceinline__ float bfhi(u32 v) { return __builtin_bit_cast(float, v & 0xffff0000u); }
__device__ __forceinline__ float rbf(float x) { return __bfloat162float(__float2bfloat16(x)); }
__device__ __forceinline__ u32 pk_bf(float lo, float hi) {
  const u32 a = __builtin_bit_cast(u16, __float2bfloat16(lo));
  const u32 b = __builtin_bit_cast(u16, __float2bfloat16(hi));
  return a | (b << 16);
}
template <int CTRL> __device__ __forceinline__ float dpp(float x) {
  return __builtin_bit_cast(float, __builtin_amdgcn_mov_dpp(__builtin_bit_cast(int, x), CTRL, 0xf, 0xf, true));
}
constexpr int XOR1 = 0xB1, XOR2 = 0x4E, XOR7 = 0x141, XOR8 = 0x128, BCAST = 0x150;

__device__ __forceinline__ float xrow_sum(float x) {
  auto s = __builtin_amdgcn_permlane16_swap(__float_as_uint(x), __float_as_uint(x), false, false);
  x = __uint_as_float(s[0]) + __uint_as_float(s[1]);
  auto t = __builtin_amdgcn_permlane32_swap(__float_as_uint(x), __float_as_uint(x), false, false);
  return __uint_as_float(t[0]) + __uint_as_float(t[1]);
}
__device__ __forceinline__ float xrow_max(float x) {
  auto s = __builtin_amdgcn_permlane16_swap(__float_as_uint(x), __float_as_uint(x), false, false);
  x = fmaxf(__uint_as_float(s[0]), __uint_as_float(s[1]));
  auto t = __builtin_amdgcn_permlane32_swap(__float_as_uint(x), __float_as_uint(x), false, false);
  return fmaxf(__uint_as_float(t[0]), __uint_as_float(t[1]));
}
__device__ __forceinline__ float row_sum(float x) {
  x += dpp<XOR1>(x); x += dpp<XOR2>(x); x += dpp<XOR7>(x); x += dpp<XOR8>(x); return x;
}
__device__ __forceinline__ float row_max(float x) {
  x = fmaxf(x, dpp<XOR1>(x)); x = fmaxf(x, dpp<XOR2>(x)); x = fmaxf(x, dpp<XOR7>(x)); x = fmaxf(x, dpp<XOR8>(x)); return x;
}
__device__ __forceinline__ float tr_reduce16(float (&x)[16], int c) {
#define TR_STEP(HALF, CTRL, BIT)                                                     \
  _Pragma("unroll") for (int h = 0; h < (HALF); ++h) {                               \
    const float lo_ = x[h], hi_ = x[h + (HALF)];                                     \
    const bool up_ = (c & (BIT)) != 0;                                               \
    const float send_ = up_ ? lo_ : hi_, keep_ = up_ ? hi_ : lo_;                    \
    x[h] = keep_ + dpp<CTRL>(send_);                                                 \
  }
  TR_STEP(8, XOR8, 8) TR_STEP(4, XOR7, 4) TR_STEP(2, XOR2, 2) TR_STEP(1, XOR1, 1)
#undef TR_STEP
  return x[0];
}

#define SCHED_FENCE() __builtin_amdgcn_sched_barrier(0)
#define STAMP(i) do {} while (0)

#define KV_NT 1
__device__ __forceinline__ u32x4 ld_nt(const u16* ub, u32 off) {
  return __builtin_nontemporal_load(reinterpret_cast<const u32x4*>(reinterpret_cast<const char*>(ub) + off));
}
#define KV_PAT 1
__device__ __forceinline__ u32x4 ld16(const u16* ub, u32 off) {
  return *reinterpret_cast<const u32x4*>(reinterpret_cast<const char*>(ub) + off);
}
__device__ __forceinline__ u32x2 ld8(const u16* ub, u32 off) {
  return *reinterpret_cast<const u32x2*>(reinterpret_cast<const char*>(ub) + off);
}
__device__ __forceinline__ u32 ld2(const u16* ub, u32 off) {
  return *reinterpret_cast<const u16*>(reinterpret_cast<const char*>(ub) + off);
}

constexpr int L_QP = 0;
constexpr int L_F1 = L_QP + NWAVE * DH;
constexpr int L_OP = L_F1 + NWAVE * 16;
constexpr int L_ML = L_OP + NWAVE * DH;
constexpr int L_RED = L_ML + NWAVE * 2;
constexpr int L_END = L_RED + NWAVE * DM;

__global__ void __launch_bounds__(512, 2)
k_main(const u16* __restrict__ x, const u16* __restrict__ kc, const u16* __restrict__ vc, const u16* __restrict__ wq,
       const u16* __restrict__ wo, const u16* __restrict__ w1, const u16* __restrict__ w2, float* __restrict__ ws) {
  __shared__ __attribute__((aligned(16))) float lds[L_END];
  const int bid = blockIdx.x, xcd = bid & 7, idx = bid >> 3;
  const int h = xcd * 4 + (idx >> 3), b = (idx >> 2) & 1, s = idx & 3;
  const int tid = threadIdx.x, lane = tid & 63, wave = __builtin_amdgcn_readfirstlane(tid >> 6);
  const int r = lane >> 4, c = lane & 15;
  const bool ffn_on = idx < 27;
  const int fg = xcd * 27 + (ffn_on ? idx : 26);

  STAMP(0);
  u32x2 xq[5];
#pragma unroll
  for (int j = 0; j < 5; ++j) xq[j] = ld8(x + b * DM + wave * 80 + 4 * j, r * 40);
  u32x4 Wq[20], KV[64], Wo[20];
  const u32x4 w1a = ld16(w1 + (size_t)(wave * 80) * FF + fg * 8, (u32)lane * (FF * 2));
  const u32x4 w1b = ld16(w1 + (size_t)(wave * 80 + 64) * FF + fg * 8, (u32)c * (FF * 2));
  const u32 xa0 = ld2(x + wave * 80, 2 * lane), xa1 = ld2(x + DM + wave * 80, 2 * lane);
  const u32 xb0 = ld2(x + wave * 80 + 64, 2 * c), xb1 = ld2(x + DM + wave * 80 + 64, 2 * c);
  SCHED_FENCE();
  constexpr int KVSTR = 16 * DH;
  const size_t kvbase = ((size_t)(b * NH + h) * SK + s * TOKWG + (wave >> 2) * (4 * TOKW) + (wave & 3) * 4) * DH;
  const u16* kp = kc + kvbase;
  const u16* vp = vc + kvbase;
  const u32 loff = lane * 16;
  {
    const u32 off = (u32)(r * 20) * (NH * DH * 2) + 16 * c;
#pragma unroll
    for (int j = 0; j < 20; ++j) {
      Wq[j] = ld16(wq + (size_t)(wave * 80 + j) * (NH * DH) + h * DH, off);
      SCHED_FENCE();
      KV[j] = ld_nt(kp + j * KVSTR, loff);
      SCHED_FENCE();
    }
  }
#pragma unroll
  for (int i = 20; i < 24; ++i) { KV[i] = ld_nt(kp + i * KVSTR, loff); SCHED_FENCE(); }

  STAMP(1);
  {
    float qa[8];
#pragma unroll
    for (int k = 0; k < 8; ++k) qa[k] = 0.f;
#pragma unroll
    for (int j = 0; j < 20; ++j) {
      const u32 xw = xq[j >> 2][(j >> 1) & 1];
      const float xv = (j & 1) ? bfhi(xw) : bflo(xw);
#pragma unroll
      for (int k = 0; k < 4; ++k) {
        qa[2 * k] = fmaf(xv, bflo(Wq[j][k]), qa[2 * k]);
        qa[2 * k + 1] = fmaf(xv, bfhi(Wq[j][k]), qa[2 * k + 1]);
      }
    }
#pragma unroll
    for (int k = 0; k < 8; ++k) qa[k] = xrow_sum(qa[k]);
    if (r == 0) {
      *reinterpret_cast<f32x4*>(&lds[L_QP + wave * DH + 8 * c]) = f32x4{qa[0], qa[1], qa[2], qa[3]};
      *reinterpret_cast<f32x4*>(&lds[L_QP + wave * DH + 8 * c + 4]) = f32x4{qa[4], qa[5], qa[6], qa[7]};
    }
    float fa[16];
    const float a0 = bflo(xa0), a1 = bflo(xa1);
    const float b0 = lane < 16 ? bflo(xb0) : 0.f, b1 = lane < 16 ? bflo(xb1) : 0.f;
#pragma unroll
    for (int k = 0; k < 4; ++k) {
      fa[2 * k] = fmaf(a0, bflo(w1a[k]), b0 * bflo(w1b[k]));
      fa[2 * k + 1] = fmaf(a0, bfhi(w1a[k]), b0 * bfhi(w1b[k]));
      fa[8 + 2 * k] = fmaf(a1, bflo(w1a[k]), b1 * bflo(w1b[k]));
      fa[8 + 2 * k + 1] = fmaf(a1, bfhi(w1a[k]), b1 * bfhi(w1b[k]));
    }
    float ft = tr_reduce16(fa, c);
    ft = xrow_sum(ft);
    if (r == 0) lds[L_F1 + wave * 16 + c] = ft;
  }
  SCHED_FENCE();
  STAMP(2);
  __syncthreads();
  STAMP(3);
  u32x2 w2r[8];
  {
    const u32 col = (tid < 160 ? tid : 0) * 8;
#pragma unroll
    for (int j = 0; j < 8; ++j) w2r[j] = ld8(w2 + (size_t)(fg * 8 + j) * DM, col);
  }
  SCHED_FENCE();
  u32 qpk[4];
  {
    f32x4 q0 = {0.f, 0.f, 0.f, 0.f}, q1 = {0.f, 0.f, 0.f, 0.f};
#pragma unroll
    for (int w = 0; w < NWAVE; ++w) {
      q0 += *reinterpret_cast<const f32x4*>(&lds[L_QP + w * DH + 8 * c]);
      q1 += *reinterpret_cast<const f32x4*>(&lds[L_QP + w * DH + 8 * c + 4]);
    }
    qpk[0] = pk_bf(q0[0], q0[1]); qpk[1] = pk_bf(q0[2], q0[3]);
    qpk[2] = pk_bf(q1[0], q1[1]); qpk[3] = pk_bf(q1[2], q1[3]);
  }
  float hval;
  {
    float t = 0.f;
#pragma unroll
    for (int w = 0; w < NWAVE; ++w) t += lds[L_F1 + w * 16 + c];
    t = rbf(t);
    hval = rbf(t / (1.f + __expf(-t)));
  }

  float sc[2];
  const u16* wop = wo + (size_t)(h * DH + wave * 16) * DM;
  const u32 wooff = (u32)r * (4 * DM * 2) + 16 * c;
#define LEADP 8
#define ISSUE_PAIR(P)                                                                               \
  do {                                                                                              \
    if ((P) >= 0 && (P) < 20) {                                                                     \
      SCHED_FENCE();                                                                                \
      _Pragma("unroll") for (int t_ = 0; t_ < 2; ++t_) {                                            \
        const int kv_ = 24 + 2 * (P) + t_;                                                          \
        KV[kv_] = ld_nt((kv_ < 32 ? kp + kv_ * KVSTR : vp + (kv_ - 32) * KVSTR), loff);             \
        SCHED_FENCE();                                                                              \
      }                                                                                             \
      Wo[P] = ld16(wop + ((P) / 5) * DM + ((P) % 5) * 128, wooff);                                  \
      SCHED_FENCE();                                                                                \
    }                                                                                               \
  } while (0)
#define LOCKSTEP 0
#define AFTER_KV(I) do { if (LOCKSTEP && (I) >= 24 && (I) < 60 && ((I) % LOCKSTEP) == LOCKSTEP - 1) { SCHED_FENCE(); __builtin_amdgcn_s_barrier(); } \
    if (((I) & 1) && (I) >= 25) ISSUE_PAIR(((I) - 25) / 2 + LEADP); } while (0)
#pragma unroll
  for (int P = 0; P < LEADP; ++P) ISSUE_PAIR(P);
#pragma unroll
  for (int g = 0; g < 2; ++g) {
    float xx[16];
#pragma unroll
    for (int i = 0; i < 16; ++i) {
      const u32x4 kk = KV[g * 16 + i];
      float d = dot2(kk[0], qpk[0], 0.f);
      d = dot2(kk[1], qpk[1], d);
      d = dot2(kk[2], qpk[2], d);
      xx[i] = dot2(kk[3], qpk[3], d);
      AFTER_KV(g * 16 + i);
    }
    STAMP(4 + g);
    const float t = tr_reduce16(xx, c);
    sc[g] = rbf(rbf(t) * SCALE);
  }
  float m = xrow_max(row_max(fmaxf(sc[0], sc[1])));
  const float m2 = m * LOG2E;
  float p[2];
  p[0] = __builtin_amdgcn_exp2f(fmaf(sc[0], LOG2E, -m2));
  p[1] = __builtin_amdgcn_exp2f(fmaf(sc[1], LOG2E, -m2));
  const float lsum = xrow_sum(row_sum(p[0] + p[1]));

  {
    float f0[4] = {0.f, 0.f, 0.f, 0.f}, f1[4] = {0.f, 0.f, 0.f, 0.f};
#define FFN2_STEP(J)                                                                  \
    {                                                                                 \
      const float h0 = dpp<BCAST + (J)>(hval), h1 = dpp<BCAST + 8 + (J)>(hval);       \
      const float w0 = bflo(w2r[J][0]), w1_ = bfhi(w2r[J][0]), w2_ = bflo(w2r[J][1]), w3 = bfhi(w2r[J][1]); \
      f0[0] = fmaf(h0, w0, f0[0]); f0[1] = fmaf(h0, w1_, f0[1]); f0[2] = fmaf(h0, w2_, f0[2]); f0[3] = fmaf(h0, w3, f0[3]); \
      f1[0] = fmaf(h1, w0, f1[0]); f1[1] = fmaf(h1, w1_, f1[1]); f1[2] = fmaf(h1, w2_, f1[2]); f1[3] = fmaf(h1, w3, f1[3]); \
    }
    FFN2_STEP(0) FFN2_STEP(1) FFN2_STEP(2) FFN2_STEP(3) FFN2_STEP(4) FFN2_STEP(5) FFN2_STEP(6) FFN2_STEP(7)
#undef FFN2_STEP
    asm volatile("" : "+v"(f0[0]), "+v"(f0[1]), "+v"(f0[2]), "+v"(f0[3]), "+v"(f1[0]), "+v"(f1[1]), "+v"(f1[2]), "+v"(f1[3]));
    if (ffn_on && tid < 160) {
      float* fp = ws + WS_F + (size_t)fg * NB * DM + tid * 4;
      *reinterpret_cast<f32x4*>(fp) = f32x4{f0[0], f0[1], f0[2], f0[3]};
      *reinterpret_cast<f32x4*>(fp + DM) = f32x4{f1[0], f1[1], f1[2], f1[3]};
    }
  }
  SCHED_FENCE();

  float acc[8];
#pragma unroll
  for (int k = 0; k < 8; ++k) acc[k] = 0.f;
#define PV_STEP(I)                                                                    \
  {                                                                                   \
    const float pb = dpp<BCAST + ((I) & 15)>(p[(I) >> 4]);                            \
    const u32x4 vv = KV[32 + (I)];                                                    \
    _Pragma("unroll") for (int k = 0; k < 4; ++k) {                                   \
      acc[2 * k] = fmaf(pb, bflo(vv[k]), acc[2 * k]);                                 \
      acc[2 * k + 1] = fmaf(pb, bfhi(vv[k]), acc[2 * k + 1]);                         \
    }                                                                                 \
    AFTER_KV(32 + (I));                                                               \
  }
  PV_STEP(0) PV_STEP(1) PV_STEP(2) PV_STEP(3) PV_STEP(4) PV_STEP(5) PV_STEP(6) PV_STEP(7)
  PV_STEP(8) PV_STEP(9) PV_STEP(10) PV_STEP(11) PV_STEP(12) PV_STEP(13) PV_STEP(14) PV_STEP(15)
  PV_STEP(16) PV_STEP(17) PV_STEP(18) PV_STEP(19) PV_STEP(20) PV_STEP(21) PV_STEP(22) PV_STEP(23)
  PV_STEP(24) PV_STEP(25) PV_STEP(26) PV_STEP(27) PV_STEP(28) PV_STEP(29) PV_STEP(30) PV_STEP(31)
#undef PV_STEP
  STAMP(6);
#pragma unroll
  for (int k = 0; k < 8; ++k) acc[k] = xrow_sum(acc[k]);
  if (r == 0) {
    *reinterpret_cast<f32x4*>(&lds[L_OP + wave * DH + 8 * c]) = f32x4{acc[0], acc[1], acc[2], acc[3]};
    *reinterpret_cast<f32x4*>(&lds[L_OP + wave * DH + 8 * c + 4]) = f32x4{acc[4], acc[5], acc[6], acc[7]};
    if (c == 0) *reinterpret_cast<f32x2*>(&lds[L_ML + wave * 2]) = f32x2{m2, lsum};
  }
  __syncthreads();

  STAMP(7);
  {
    float mw[8], lw[8];
#pragma unroll
    for (int w = 0; w < 4; ++w) {
      const f32x4 t = *reinterpret_cast<const f32x4*>(&lds[L_ML + 4 * w]);
      mw[2 * w] = t[0]; lw[2 * w] = t[1]; mw[2 * w + 1] = t[2]; lw[2 * w + 1] = t[3];
    }
    float M = mw[0];
#pragma unroll
    for (int w = 1; w < 8; ++w) M = fmaxf(M, mw[w]);
    f32x4 o4 = {0.f, 0.f, 0.f, 0.f};
    float L = 0.f;
#pragma unroll
    for (int w = 0; w < 8; ++w) {
      const float e = __builtin_amdgcn_exp2f(mw[w] - M);
      L = fmaf(e, lw[w], L);
      o4 += e * *reinterpret_cast<const f32x4*>(&lds[L_OP + w * DH + (wave * 4 + r) * 4]);
    }
    if (tid == 0) *reinterpret_cast<f32x2*>(ws + WS_ML + (size_t)((b * NH + h) * NSPLIT + s) * 2) = f32x2{M, L};
    float y[40];
#pragma unroll
    for (int k = 0; k < 40; ++k) y[k] = 0.f;
#pragma unroll
    for (int i = 0; i < 20; ++i) {
      const float ov = o4[i / 5];
      const int seg = i % 5;
      const u32x4 ww = Wo[i];
#pragma unroll
      for (int k = 0; k < 4; ++k) {
        y[(2 * k) * 5 + seg] = fmaf(ov, bflo(ww[k]), y[(2 * k) * 5 + seg]);
        y[(2 * k + 1) * 5 + seg] = fmaf(ov, bfhi(ww[k]), y[(2 * k + 1) * 5 + seg]);
      }
    }
    float z[20];
#pragma unroll
    for (int v = 0; v < 20; ++v) {
      auto t = __builtin_amdgcn_permlane32_swap(__float_as_uint(y[v]), __float_as_uint(y[v + 20]), false, false);
      z[v] = __uint_as_float(t[0]) + __uint_as_float(t[1]);
    }
    float u[10];
#pragma unroll
    for (int v = 0; v < 10; ++v) {
      auto t = __builtin_amdgcn_permlane16_swap(__float_as_uint(z[v]), __float_as_uint(z[v + 10]), false, false);
      u[v] = __uint_as_float(t[0]) + __uint_as_float(t[1]);
    }
#pragma unroll
    for (int seg = 0; seg < 5; ++seg)
      *reinterpret_cast<f32x2*>(&lds[L_RED + wave * DM + seg * 128 + 8 * c + 2 * r]) = f32x2{u[seg], u[5 + seg]};
  }
  __syncthreads();
  if (tid < 160) {
    f32x4 t = {0.f, 0.f, 0.f, 0.f};
#pragma unroll
    for (int w = 0; w < NWAVE; ++w) t += *reinterpret_cast<const f32x4*>(&lds[L_RED + w * DM + tid * 4]);
    *reinterpret_cast<f32x4*>(ws + WS_P + (size_t)((b * NH + h) * NSPLIT + s) * DM + tid * 4) = t;
  }
}

__global__ void __launch_bounds__(256)
k_fin(const u16* __restrict__ x, const u16* __restrict__ gw, const float* __restrict__ ws, u16* __restrict__ out) {
  __shared__ __attribute__((aligned(16))) float red[4][2][32];
  const int b = blockIdx.x / 20, cb = blockIdx.x % 20;
  const int tid = threadIdx.x, lane = tid & 63, wave = tid >> 6;
  const int c4 = tid & 7, rg = tid >> 3;
  const int col = cb * 32 + c4 * 4;
  f32x4 pr[4];
#pragma unroll
  for (int s = 0; s < 4; ++s)
    pr[s] = *reinterpret_cast<const f32x4*>(ws + WS_P + (size_t)((b * NH + rg) * NSPLIT + s) * DM + col);
  const f32x4 ml0 = *reinterpret_cast<const f32x4*>(ws + WS_ML + (size_t)(b * NH + rg) * NSPLIT * 2);
  const f32x4 ml1 = *reinterpret_cast<const f32x4*>(ws + WS_ML + (size_t)(b * NH + rg) * NSPLIT * 2 + 4);
  f32x4 fr[7];
#pragma unroll
  for (int j = 0; j < 7; ++j) {
    const int f = rg + 32 * j;
    const int fc = f < NFG ? f : NFG - 1;
    fr[j] = *reinterpret_cast<const f32x4*>(ws + WS_F + (size_t)(fc * NB + b) * DM + col);
  }
  u32 xg[5], gg[5];
#pragma unroll
  for (int j = 0; j < 5; ++j) {
    xg[j] = *reinterpret_cast<const u32*>(x + b * DM + 2 * (lane + 64 * j));
    gg[j] = *reinterpret_cast<const u32*>(gw + 2 * (lane + 64 * j));
  }
  float gs = 0.f;
#pragma unroll
  for (int j = 0; j < 5; ++j) gs += rbf(bflo(xg[j]) * bflo(gg[j])) + rbf(bfhi(xg[j]) * bfhi(gg[j]));
  gs = xrow_sum(row_sum(gs));
  gs = rbf(gs);
  const float g = rbf(1.f / (1.f + __expf(-gs)));
  const float M = fmaxf(fmaxf(ml0[0], ml0[2]), fmaxf(ml1[0], ml1[2]));
  const float e0 = __builtin_amdgcn_exp2f(ml0[0] - M), e1 = __builtin_amdgcn_exp2f(ml0[2] - M);
  const float e2 = __builtin_amdgcn_exp2f(ml1[0] - M), e3 = __builtin_amdgcn_exp2f(ml1[2] - M);
  const float L = e0 * ml0[1] + e1 * ml0[3] + e2 * ml1[1] + e3 * ml1[3];
  const float inv = 1.f / L;
  f32x4 a = (e0 * inv) * pr[0] + (e1 * inv) * pr[1] + (e2 * inv) * pr[2] + (e3 * inv) * pr[3];
  f32x4 f = fr[0] + fr[1] + fr[2] + fr[3] + fr[4] + fr[5];
  if (rg + 32 * 6 < NFG) f += fr[6];
#pragma unroll
  for (int k = 0; k < 4; ++k) {
    a[k] += dpp<XOR8>(a[k]); a[k] = xrow_sum(a[k]);
    f[k] += dpp<XOR8>(f[k]); f[k] = xrow_sum(f[k]);
  }
  if (lane < 8) {
    *reinterpret_cast<f32x4*>(&red[wave][0][c4 * 4]) = a;
    *reinterpret_cast<f32x4*>(&red[wave][1][c4 * 4]) = f;
  }
  __syncthreads();
  if (tid < 32) {
    const float av = rbf(red[0][0][tid] + red[1][0][tid] + red[2][0][tid] + red[3][0][tid]);
    const float fv = rbf(red[0][1][tid] + red[1][1][tid] + red[2][1][tid] + red[3][1][tid]);
    const float t1 = rbf(g * av), t2 = rbf(rbf(1.f - g) * fv);
    out[b * DM + cb * 32 + tid] = __builtin_bit_cast(u16, __float2bfloat16(t1 + t2));
  }
}

}

extern "C" void kernel_launch(void* const* d_in, const int* in_sizes, int n_in, void* d_out, int out_size, void* d_ws,
                              size_t ws_size, hipStream_t stream) {
  const u16* x = (const u16*)d_in[0];
  const u16* kc = (const u16*)d_in[1];
  const u16* vc = (const u16*)d_in[2];
  const u16* wq = (const u16*)d_in[3];
  const u16* wo = (const u16*)d_in[4];
  const u16* w1 = (const u16*)d_in[6];
  const u16* w2 = (const u16*)d_in[7];
  const u16* gw = (const u16*)d_in[8];
  float* ws = (float*)d_ws;
  k_main<<<dim3(NB * NH * NSPLIT), dim3(512), 0, stream>>>(x, kc, vc, wq, wo, w1, w2, ws);
  k_fin<<<dim3(NB * 20), dim3(256), 0, stream>>>(x, gw, ws, (u16*)d_out);
}
```

```cpp
#define W_NT 0
#define ABAR 0
#define PRE 5
#include <hip/hip_runtime.h>
#include <hip/hip_bf16.h>
#include <stdint.h>

typedef unsigned int u32;
typedef unsigned short u16;
typedef __attribute__((ext_vector_type(4))) unsigned u32x4;
typedef __attribute__((ext_vector_type(2))) unsigned u32x2;
typedef __attribute__((ext_vector_type(4))) float f32x4;
typedef __attribute__((ext_vector_type(2))) float f32x2;
typedef __attribute__((ext_vector_type(2))) __bf16 bf16x2;

namespace {

constexpr int NB = 2, DM = 640, NH = 32, SK = 4096, DH = 128, FF = 1728;
constexpr int NSPLIT = 4, TOKWG = SK / NSPLIT, NWAVE = 8, TOKW = TOKWG / NWAVE;
constexpr int NFG = FF / 8;
constexpr float LOG2E = 1.4426950408889634f;
constexpr float SCALE = 0.088388347648318440f;

constexpr size_t WS_P = 0;
constexpr size_t WS_ML = WS_P + (size_t)NB * NH * NSPLIT * DM;
constexpr size_t WS_F = WS_ML + (size_t)NB * NH * NSPLIT * 2;

__device__ __forceinline__ float dot2(u32 a, u32 b, float acc) {
  return __builtin_amdgcn_fdot2_f32_bf16(__builtin_bit_cast(bf16x2, a), __builtin_bit_cast(bf16x2, b), acc, false);
}
__device__ __forceinline__ float bflo(u32 v) { return __builtin_bit_cast(float, v << 16); }
__device__ __forceinline__ float bfhi(u32 v) { return __builtin_bit_cast(float, v & 0xffff0000u); }
__device__ __forceinline__ float rbf(float x) { return __bfloat162float(__float2bfloat16(x)); }
__device__ __forceinline__ u32 pk_bf(float lo, float hi) {
  const u32 a = __builtin_bit_cast(u16, __float2bfloat16(lo));
  const u32 b = __builtin_bit_cast(u16, __float2bfloat16(hi));
  return a | (b << 16);
}
template <int CTRL> __device__ __forceinline__ float dpp(float x) {
  return __builtin_bit_cast(float, __builtin_amdgcn_mov_dpp(__builtin_bit_cast(int, x), CTRL, 0xf, 0xf, true));
}
constexpr int XOR1 = 0xB1, XOR2 = 0x4E, XOR7 = 0x141, XOR8 = 0x128, BCAST = 0x150;

__device__ __forceinline__ float xrow_sum(float x) {
  auto s = __builtin_amdgcn_permlane16_swap(__float_as_uint(x), __float_as_uint(x), false, false);
  x = __uint_as_float(s[0]) + __uint_as_float(s[1]);
  auto t = __builtin_amdgcn_permlane32_swap(__float_as_uint(x), __float_as_uint(x), false, false);
  return __uint_as_float(t[0]) + __uint_as_float(t[1]);
}
__device__ __forceinline__ float xrow_max(float x) {
  auto s = __builtin_amdgcn_permlane16_swap(__float_as_uint(x), __float_as_uint(x), false, false);
  x = fmaxf(__uint_as_float(s[0]), __uint_as_float(s[1]));
  auto t = __builtin_amdgcn_permlane32_swap(__float_as_uint(x), __float_as_uint(x), false, false);
  return fmaxf(__uint_as_float(t[0]), __uint_as_float(t[1]));
}
__device__ __forceinline__ float row_sum(float x) {
  x += dpp<XOR1>(x); x += dpp<XOR2>(x); x += dpp<XOR7>(x); x += dpp<XOR8>(x); return x;
}
__device__ __forceinline__ float row_max(float x) {
  x = fmaxf(x, dpp<XOR1>(x)); x = fmaxf(x, dpp<XOR2>(x)); x = fmaxf(x, dpp<XOR7>(x)); x = fmaxf(x, dpp<XOR8>(x)); return x;
}
__device__ __forceinline__ float tr_reduce16(float (&x)[16], int c) {
#define TR_STEP(HALF, CTRL, BIT)                                                     \
  _Pragma("unroll") for (int h = 0; h < (HALF); ++h) {                               \
    const float lo_ = x[h], hi_ = x[h + (HALF)];                                     \
    const bool up_ = (c & (BIT)) != 0;                                               \
    const float send_ = up_ ? lo_ : hi_, keep_ = up_ ? hi_ : lo_;                    \
    x[h] = keep_ + dpp<CTRL>(send_);                                                 \
  }
  TR_STEP(8, XOR8, 8) TR_STEP(4, XOR7, 4) TR_STEP(2, XOR2, 2) TR_STEP(1, XOR1, 1)
#undef TR_STEP
  return x[0];
}

#define SCHED_FENCE() __builtin_amdgcn_sched_barrier(0)
#define STAMP(i) do {} while (0)

#define KV_NT 1
__device__ __forceinline__ u32x4 ld_nt(const u16* ub, u32 off) {
  return __builtin_nontemporal_load(reinterpret_cast<const u32x4*>(reinterpret_cast<const char*>(ub) + off));
}
#define KV_PAT 1
__device__ __forceinline__ u32x4 ld16(const u16* ub, u32 off) {
  return *reinterpret_cast<const u32x4*>(reinterpret_cast<const char*>(ub) + off);
}
__device__ __forceinline__ u32x2 ld8(const u16* ub, u32 off) {
  return *reinterpret_cast<const u32x2*>(reinterpret_cast<const char*>(ub) + off);
}
__device__ __forceinline__ u32 ld2(const u16* ub, u32 off) {
  return *reinterpret_cast<const u16*>(reinterpret_cast<const char*>(ub) + off);
}

constexpr int L_QP = 0;
constexpr int L_F1 = L_QP + NWAVE * DH;
constexpr int L_OP = L_F1 + NWAVE * 16;
constexpr int L_ML = L_OP + NWAVE * DH;
constexpr int L_RED = L_ML + NWAVE * 2;
constexpr int L_END = L_RED + NWAVE * DM;

__global__ void __launch_bounds__(512, 2)
k_main(const u16* __restrict__ x, const u16* __restrict__ kc, const u16* __restrict__ vc, const u16* __restrict__ wq,
       const u16* __restrict__ wo, const u16* __restrict__ w1, const u16* __restrict__ w2, float* __restrict__ ws) {
  __shared__ __attribute__((aligned(16))) float lds[L_END];
  const int bid = blockIdx.x, xcd = bid & 7, idx = bid >> 3;
  const int h = xcd * 4 + (idx >> 3), b = (idx >> 2) & 1, s = idx & 3;
  const int tid = threadIdx.x, lane = tid & 63, wave = __builtin_amdgcn_readfirstlane(tid >> 6);
  const int r = lane >> 4, c = lane & 15;
  const bool ffn_on = idx < 27;
  const int fg = xcd * 27 + (ffn_on ? idx : 26);

  STAMP(0);
  u32x2 xq[5];
#pragma unroll
  for (int j = 0; j < 5; ++j) xq[j] = ld8(x + b * DM + wave * 80 + 4 * j, r * 40);
  SCHED_FENCE();
  u32x4 Wq[20], KV[64], Wo[20];
  constexpr int KVSTR = 16 * DH;
  const size_t kvbase = ((size_t)(b * NH + h) * SK + s * TOKWG + (wave >> 2) * (4 * TOKW) + (wave & 3) * 4) * DH;
  const u16* kp = kc + kvbase;
  const u16* vp = vc + kvbase;
  const u32 loff = lane * 16;
  {
    const u32 off = (u32)(r * 20) * (NH * DH * 2) + 16 * c;
#pragma unroll
    for (int j = 0; j < 20; ++j) {
      Wq[j] = ld16(wq + (size_t)(wave * 80 + j) * (NH * DH) + h * DH, off);
      SCHED_FENCE();
      KV[j] = ld_nt(kp + j * KVSTR, loff);
      SCHED_FENCE();
    }
  }
#pragma unroll
  for (int i = 20; i < 24; ++i) { KV[i] = ld_nt(kp + i * KVSTR, loff); SCHED_FENCE(); }
  const u16* wop = wo + (size_t)(h * DH + wave * 16) * DM;
  const u32 wooff = (u32)r * (4 * DM * 2) + 16 * c;
#define ISSUE_T(T)                                                                                  \
  do {                                                                                              \
    if ((T) >= 0 && (T) < 20) {                                                                     \
      SCHED_FENCE();                                                                                \
      Wo[T] = ld16(wop + (2 * (((T) >> 1) / 5) + ((T) & 1)) * DM + (((T) >> 1) % 5) * 128, wooff);  \
      SCHED_FENCE();                                                                                \
      _Pragma("unroll") for (int t_ = 0; t_ < 2; ++t_) {                                            \
        const int kv_ = 24 + 2 * (T) + t_;                                                          \
        KV[kv_] = ld_nt((kv_ < 32 ? kp + kv_ * KVSTR : vp + (kv_ - 32) * KVSTR), loff);             \
        SCHED_FENCE();                                                                              \
      }                                                                                             \
    }                                                                                               \
  } while (0)
#define PERM_Q(Q)                                                                                   \
  do {                                                                                              \
    _Pragma("unroll") for (int k_ = 0; k_ < 4; ++k_) {                                              \
      const u32 a_ = Wo[2 * (Q)][k_], b_ = Wo[2 * (Q) + 1][k_];                                     \
      Wo[2 * (Q)][k_] = __builtin_amdgcn_perm(b_, a_, 0x05040100u);                                 \
      Wo[2 * (Q) + 1][k_] = __builtin_amdgcn_perm(b_, a_, 0x07060302u);                             \
    }                                                                                               \
  } while (0)
#define AFTER_KV(I)                                                                                 \
  do {                                                                                              \
    if ((I) % 3 == 2) ISSUE_T(PRE + (I) / 3);                                                       \
    if ((I) >= 26 && ((I) - 26) % 4 == 0) PERM_Q(((I) - 26) / 4);                                   \
  } while (0)

  STAMP(1);
  {
    float qa[8];
#pragma unroll
    for (int k = 0; k < 8; ++k) qa[k] = 0.f;
#pragma unroll
    for (int j = 0; j < 20; ++j) {
      const u32 xw = xq[j >> 2][(j >> 1) & 1];
      const float xv = (j & 1) ? bfhi(xw) : bflo(xw);
#pragma unroll
      for (int k = 0; k < 4; ++k) {
        qa[2 * k] = fmaf(xv, bflo(Wq[j][k]), qa[2 * k]);
        qa[2 * k + 1] = fmaf(xv, bfhi(Wq[j][k]), qa[2 * k + 1]);
      }
    }
#pragma unroll
    for (int k = 0; k < 8; ++k) qa[k] = xrow_sum(qa[k]);
    if (r == 0) {
      *reinterpret_cast<f32x4*>(&lds[L_QP + wave * DH + 8 * c]) = f32x4{qa[0], qa[1], qa[2], qa[3]};
      *reinterpret_cast<f32x4*>(&lds[L_QP + wave * DH + 8 * c + 4]) = f32x4{qa[4], qa[5], qa[6], qa[7]};
    }
  }
  SCHED_FENCE();
  STAMP(2);
  const u32x4 w1a = ld16(w1 + (size_t)(wave * 80) * FF + fg * 8, (u32)lane * (FF * 2));
  const u32x4 w1b = ld16(w1 + (size_t)(wave * 80 + 64) * FF + fg * 8, (u32)c * (FF * 2));
  const u32 xa0 = ld2(x + wave * 80, 2 * lane), xa1 = ld2(x + DM + wave * 80, 2 * lane);
  const u32 xb0 = ld2(x + wave * 80 + 64, 2 * c), xb1 = ld2(x + DM + wave * 80 + 64, 2 * c);
  SCHED_FENCE();
#pragma unroll
  for (int T = 0; T < PRE; ++T) ISSUE_T(T);
  __syncthreads();
  STAMP(3);
  u32 qpk[4];
  {
    f32x4 q0 = {0.f, 0.f, 0.f, 0.f}, q1 = {0.f, 0.f, 0.f, 0.f};
#pragma unroll
    for (int w = 0; w < NWAVE; ++w) {
      q0 += *reinterpret_cast<const f32x4*>(&lds[L_QP + w * DH + 8 * c]);
      q1 += *reinterpret_cast<const f32x4*>(&lds[L_QP + w * DH + 8 * c + 4]);
    }
    qpk[0] = pk_bf(q0[0], q0[1]); qpk[1] = pk_bf(q0[2], q0[3]);
    qpk[2] = pk_bf(q1[0], q1[1]); qpk[3] = pk_bf(q1[2], q1[3]);
  }

  float sc[2];
  u32x2 w2r[8];
#pragma unroll
  for (int g = 0; g < 2; ++g) {
    float xx[16];
#pragma unroll
    for (int i = 0; i < 16; ++i) {
      const u32x4 kk = KV[g * 16 + i];
      float d = dot2(kk[0], qpk[0], 0.f);
      d = dot2(kk[1], qpk[1], d);
      d = dot2(kk[2], qpk[2], d);
      xx[i] = dot2(kk[3], qpk[3], d);
      AFTER_KV(g * 16 + i);
    }
    STAMP(4 + g);
    const float t = tr_reduce16(xx, c);
    sc[g] = rbf(rbf(t) * SCALE);
    if (g == 0) {
      SCHED_FENCE();
      float fa[16];
      const float a0 = bflo(xa0), a1 = bflo(xa1);
      const float b0 = lane < 16 ? bflo(xb0) : 0.f, b1 = lane < 16 ? bflo(xb1) : 0.f;
#pragma unroll
      for (int k = 0; k < 4; ++k) {
        fa[2 * k] = fmaf(a0, bflo(w1a[k]), b0 * bflo(w1b[k]));
        fa[2 * k + 1] = fmaf(a0, bfhi(w1a[k]), b0 * bfhi(w1b[k]));
        fa[8 + 2 * k] = fmaf(a1, bflo(w1a[k]), b1 * bflo(w1b[k]));
        fa[8 + 2 * k + 1] = fmaf(a1, bfhi(w1a[k]), b1 * bfhi(w1b[k]));
      }
      float ft = tr_reduce16(fa, c);
      ft = xrow_sum(ft);
      if (r == 0) lds[L_F1 + wave * 16 + c] = ft;
      SCHED_FENCE();
      const u32 col = (tid < 160 ? tid : 0) * 8;
#pragma unroll
      for (int j = 0; j < 8; ++j) w2r[j] = ld8(w2 + (size_t)(fg * 8 + j) * DM, col);
      SCHED_FENCE();
    }
  }
  {
    const float mw = xrow_max(row_max(fmaxf(sc[0], sc[1])));
    if (lane == 0) lds[L_ML + wave] = mw;
  }
  __syncthreads();
  float m2;
  {
    const f32x4 t0 = *reinterpret_cast<const f32x4*>(&lds[L_ML]), t1 = *reinterpret_cast<const f32x4*>(&lds[L_ML + 4]);
    m2 = fmaxf(fmaxf(fmaxf(t0[0], t0[1]), fmaxf(t0[2], t0[3])), fmaxf(fmaxf(t1[0], t1[1]), fmaxf(t1[2], t1[3]))) * LOG2E;
  }
  float p[2];
  p[0] = __builtin_amdgcn_exp2f(fmaf(sc[0], LOG2E, -m2));
  p[1] = __builtin_amdgcn_exp2f(fmaf(sc[1], LOG2E, -m2));
  const float lsum = xrow_sum(row_sum(p[0] + p[1]));
  if (lane == 0) lds[L_ML + 8 + wave] = lsum;

  {
    float hval;
    float t = 0.f;
#pragma unroll
    for (int w = 0; w < NWAVE; ++w) t += lds[L_F1 + w * 16 + c];
    t = rbf(t);
    hval = rbf(t / (1.f + __expf(-t)));
    float f0[4] = {0.f, 0.f, 0.f, 0.f}, f1[4] = {0.f, 0.f, 0.f, 0.f};
#define FFN2_STEP(J)                                                                  \
    {                                                                                 \
      const float h0 = dpp<BCAST + (J)>(hval), h1 = dpp<BCAST + 8 + (J)>(hval);       \
      const float w0 = bflo(w2r[J][0]), w1_ = bfhi(w2r[J][0]), w2_ = bflo(w2r[J][1]), w3 = bfhi(w2r[J][1]); \
      f0[0] = fmaf(h0, w0, f0[0]); f0[1] = fmaf(h0, w1_, f0[1]); f0[2] = fmaf(h0, w2_, f0[2]); f0[3] = fmaf(h0, w3, f0[3]); \
      f1[0] = fmaf(h1, w0, f1[0]); f1[1] = fmaf(h1, w1_, f1[1]); f1[2] = fmaf(h1, w2_, f1[2]); f1[3] = fmaf(h1, w3, f1[3]); \
    }
    FFN2_STEP(0) FFN2_STEP(1) FFN2_STEP(2) FFN2_STEP(3) FFN2_STEP(4) FFN2_STEP(5) FFN2_STEP(6) FFN2_STEP(7)
#undef FFN2_STEP
    asm volatile("" : "+v"(f0[0]), "+v"(f0[1]), "+v"(f0[2]), "+v"(f0[3]), "+v"(f1[0]), "+v"(f1[1]), "+v"(f1[2]), "+v"(f1[3]));
    if (ffn_on && tid < 160) {
      float* fp = ws + WS_F + (size_t)fg * NB * DM + tid * 4;
      *reinterpret_cast<f32x4*>(fp) = f32x4{f0[0], f0[1], f0[2], f0[3]};
      *reinterpret_cast<f32x4*>(fp + DM) = f32x4{f1[0], f1[1], f1[2], f1[3]};
    }
  }
  SCHED_FENCE();

  float acc[8];
#pragma unroll
  for (int k = 0; k < 8; ++k) acc[k] = 0.f;
#define PV_STEP(I)                                                                    \
  {                                                                                   \
    const float pb = dpp<BCAST + ((I) & 15)>(p[(I) >> 4]);                            \
    const u32x4 vv = KV[32 + (I)];                                                    \
    _Pragma("unroll") for (int k = 0; k < 4; ++k) {                                   \
      acc[2 * k] = fmaf(pb, bflo(vv[k]), acc[2 * k]);                                 \
      acc[2 * k + 1] = fmaf(pb, bfhi(vv[k]), acc[2 * k + 1]);                         \
    }                                                                                 \
    AFTER_KV(32 + (I));                                                               \
  }
  PV_STEP(0) PV_STEP(1) PV_STEP(2) PV_STEP(3) PV_STEP(4) PV_STEP(5) PV_STEP(6) PV_STEP(7)
  PV_STEP(8) PV_STEP(9) PV_STEP(10) PV_STEP(11) PV_STEP(12) PV_STEP(13) PV_STEP(14) PV_STEP(15)
  PV_STEP(16) PV_STEP(17) PV_STEP(18) PV_STEP(19) PV_STEP(20) PV_STEP(21) PV_STEP(22) PV_STEP(23)
  PV_STEP(24) PV_STEP(25) PV_STEP(26) PV_STEP(27) PV_STEP(28) PV_STEP(29) PV_STEP(30) PV_STEP(31)
#undef PV_STEP
  STAMP(6);
#pragma unroll
  for (int k = 0; k < 8; ++k) acc[k] = xrow_sum(acc[k]);
  if (r == 0) {
    *reinterpret_cast<f32x4*>(&lds[L_OP + wave * DH + 8 * c]) = f32x4{acc[0], acc[1], acc[2], acc[3]};
    *reinterpret_cast<f32x4*>(&lds[L_OP + wave * DH + 8 * c + 4]) = f32x4{acc[4], acc[5], acc[6], acc[7]};
  }
  __syncthreads();

  STAMP(7);
  {
    f32x4 o4 = {0.f, 0.f, 0.f, 0.f};
#pragma unroll
    for (int w = 0; w < 8; ++w) o4 += *reinterpret_cast<const f32x4*>(&lds[L_OP + w * DH + (wave * 4 + r) * 4]);
    if (tid == 0) {
      const f32x4 t0 = *reinterpret_cast<const f32x4*>(&lds[L_ML + 8]), t1 = *reinterpret_cast<const f32x4*>(&lds[L_ML + 12]);
      const float L = ((t0[0] + t0[1]) + (t0[2] + t0[3])) + ((t1[0] + t1[1]) + (t1[2] + t1[3]));
      *reinterpret_cast<f32x2*>(ws + WS_ML + (size_t)((b * NH + h) * NSPLIT + s) * 2) = f32x2{m2, L};
    }
    const u32 o01 = pk_bf(o4[0], o4[1]), o23 = pk_bf(o4[2], o4[3]);
    float y[40];
#pragma unroll
    for (int seg = 0; seg < 5; ++seg)
#pragma unroll
      for (int k = 0; k < 4; ++k) {
        y[(2 * k) * 5 + seg] = dot2(Wo[2 * (5 + seg)][k], o23, dot2(Wo[2 * seg][k], o01, 0.f));
        y[(2 * k + 1) * 5 + seg] = dot2(Wo[2 * (5 + seg) + 1][k], o23, dot2(Wo[2 * seg + 1][k], o01, 0.f));
      }
    float z[20];
#pragma unroll
    for (int v = 0; v < 20; ++v) {
      auto t = __builtin_amdgcn_permlane32_swap(__float_as_uint(y[v]), __float_as_uint(y[v + 20]), false, false);
      z[v] = __uint_as_float(t[0]) + __uint_as_float(t[1]);
    }
    float u[10];
#pragma unroll
    for (int v = 0; v < 10; ++v) {
      auto t = __builtin_amdgcn_permlane16_swap(__float_as_uint(z[v]), __float_as_uint(z[v + 10]), false, false);
      u[v] = __uint_as_float(t[0]) + __uint_as_float(t[1]);
    }
#pragma unroll
    for (int seg = 0; seg < 5; ++seg)
      *reinterpret_cast<f32x2*>(&lds[L_RED + wave * DM + seg * 128 + 8 * c + 2 * r]) = f32x2{u[seg], u[5 + seg]};
  }
  __syncthreads();
  if (tid < 160) {
    f32x4 t = {0.f, 0.f, 0.f, 0.f};
#pragma unroll
    for (int w = 0; w < NWAVE; ++w) t += *reinterpret_cast<const f32x4*>(&lds[L_RED + w * DM + tid * 4]);
    *reinterpret_cast<f32x4*>(ws + WS_P + (size_t)((b * NH + h) * NSPLIT + s) * DM + tid * 4) = t;
  }
}

__global__ void __launch_bounds__(256)
k_fin(const u16* __restrict__ x, const u16* __restrict__ gw, const float* __restrict__ ws, u16* __restrict__ out) {
  __shared__ __attribute__((aligned(16))) float red[4][2][32];
  const int b = blockIdx.x / 20, cb = blockIdx.x % 20;
  const int tid = threadIdx.x, lane = tid & 63, wave = tid >> 6;
  const int c4 = tid & 7, rg = tid >> 3;
  const int col = cb * 32 + c4 * 4;
  f32x4 pr[4];
#pragma unroll
  for (int s = 0; s < 4; ++s)
    pr[s] = *reinterpret_cast<const f32x4*>(ws + WS_P + (size_t)((b * NH + rg) * NSPLIT + s) * DM + col);
  const f32x4 ml0 = *reinterpret_cast<const f32x4*>(ws + WS_ML + (size_t)(b * NH + rg) * NSPLIT * 2);
  const f32x4 ml1 = *reinterpret_cast<const f32x4*>(ws + WS_ML + (size_t)(b * NH + rg) * NSPLIT * 2 + 4);
  f32x4 fr[7];
#pragma unroll
  for (int j = 0; j < 7; ++j) {
    const int f = rg + 32 * j;
    const int fc = f < NFG ? f : NFG - 1;
    fr[j] = *reinterpret_cast<const f32x4*>(ws + WS_F + (size_t)(fc * NB + b) * DM + col);
  }
  u32 xg[5], gg[5];
#pragma unroll
  for (int j = 0; j < 5; ++j) {
    xg[j] = *reinterpret_cast<const u32*>(x + b * DM + 2 * (lane + 64 * j));
    gg[j] = *reinterpret_cast<const u32*>(gw + 2 * (lane + 64 * j));
  }
  float gs = 0.f;
#pragma unroll
  for (int j = 0; j < 5; ++j) gs += rbf(bflo(xg[j]) * bflo(gg[j])) + rbf(bfhi(xg[j]) * bfhi(gg[j]));
  gs = xrow_sum(row_sum(gs));
  gs = rbf(gs);
  const float g = rbf(1.f / (1.f + __expf(-gs)));
  const float M = fmaxf(fmaxf(ml0[0], ml0[2]), fmaxf(ml1[0], ml1[2]));
  const float e0 = __builtin_amdgcn_exp2f(ml0[0] - M), e1 = __builtin_amdgcn_exp2f(ml0[2] - M);
  const float e2 = __builtin_amdgcn_exp2f(ml1[0] - M), e3 = __builtin_amdgcn_exp2f(ml1[2] - M);
  const float L = e0 * ml0[1] + e1 * ml0[3] + e2 * ml1[1] + e3 * ml1[3];
  const float inv = 1.f / L;
  f32x4 a = (e0 * inv) * pr[0] + (e1 * inv) * pr[1] + (e2 * inv) * pr[2] + (e3 * inv) * pr[3];
  f32x4 f = fr[0] + fr[1] + fr[2] + fr[3] + fr[4] + fr[5];
  if (rg + 32 * 6 < NFG) f += fr[6];
#pragma unroll
  for (int k = 0; k < 4; ++k) {
    a[k] += dpp<XOR8>(a[k]); a[k] = xrow_sum(a[k]);
    f[k] += dpp<XOR8>(f[k]); f[k] = xrow_sum(f[k]);
  }
  if (lane < 8) {
    *reinterpret_cast<f32x4*>(&red[wave][0][c4 * 4]) = a;
    *reinterpret_cast<f32x4*>(&red[wave][1][c4 * 4]) = f;
  }
  __syncthreads();
  if (tid < 32) {
    const float av = rbf(red[0][0][tid] + red[1][0][tid] + red[2][0][tid] + red[3][0][tid]);
    const float fv = rbf(red[0][1][tid] + red[1][1][tid] + red[2][1][tid] + red[3][1][tid]);
    const float t1 = rbf(g * av), t2 = rbf(rbf(1.f - g) * fv);
    out[b * DM + cb * 32 + tid] = __builtin_bit_cast(u16, __float2bfloat16(t1 + t2));
  }
}

}

extern "C" void kernel_launch(void* const* d_in, const int* in_sizes, int n_in, void* d_out, int out_size, void* d_ws,
                              size_t ws_size, hipStream_t stream) {
  const u16* x = (const u16*)d_in[0];
  const u16* kc = (const u16*)d_in[1];
  const u16* vc = (const u16*)d_in[2];
  const u16* wq = (const u16*)d_in[3];
  const u16* wo = (const u16*)d_in[4];
  const u16* w1 = (const u16*)d_in[6];
  const u16* w2 = (const u16*)d_in[7];
  const u16* gw = (const u16*)d_in[8];
  float* ws = (float*)d_ws;
  k_main<<<dim3(NB * NH * NSPLIT), dim3(512), 0, stream>>>(x, kc, vc, wq, wo, w1, w2, ws);
  k_fin<<<dim3(NB * 20), dim3(256), 0, stream>>>(x, gw, ws, (u16*)d_out);
}
```

```cpp
#define TLBPF 0
#include <hip/hip_runtime.h>
#include <hip/hip_bf16.h>
#include <stdint.h>

typedef unsigned int u32;
typedef unsigned short u16;
typedef __attribute__((ext_vector_type(4))) unsigned u32x4;
typedef __attribute__((ext_vector_type(2))) unsigned u32x2;
typedef __attribute__((ext_vector_type(4))) float f32x4;
typedef __attribute__((ext_vector_type(2))) float f32x2;
typedef __attribute__((ext_vector_type(2))) __bf16 bf16x2;

namespace {

constexpr int NB = 2, DM = 640, NH = 32, SK = 4096, DH = 128, FF = 1728;
constexpr int NSPLIT = 4, TOKWG = SK / NSPLIT, NWAVE = 8, TOKW = TOKWG / NWAVE;
constexpr int NFG = FF / 8;
constexpr float LOG2E = 1.4426950408889634f;
constexpr float SCALE = 0.088388347648318440f;

constexpr size_t WS_P = 0;
constexpr size_t WS_ML = WS_P + (size_t)NB * NH * NSPLIT * DM;
constexpr size_t WS_F = WS_ML + (size_t)NB * NH * NSPLIT * 2;

__device__ __forceinline__ float dot2(u32 a, u32 b, float acc) {
  return __builtin_amdgcn_fdot2_f32_bf16(__builtin_bit_cast(bf16x2, a), __builtin_bit_cast(bf16x2, b), acc, false);
}
__device__ __forceinline__ float bflo(u32 v) { return __builtin_bit_cast(float, v << 16); }
__device__ __forceinline__ float bfhi(u32 v) { return __builtin_bit_cast(float, v & 0xffff0000u); }
__device__ __forceinline__ float rbf(float x) { return __bfloat162float(__float2bfloat16(x)); }
__device__ __forceinline__ u32 pk_bf(float lo, float hi) {
  const u32 a = __builtin_bit_cast(u16, __float2bfloat16(lo));
  const u32 b = __builtin_bit_cast(u16, __float2bfloat16(hi));
  return a | (b << 16);
}
template <int CTRL> __device__ __forceinline__ float dpp(float x) {
  return __builtin_bit_cast(float, __builtin_amdgcn_mov_dpp(__builtin_bit_cast(int, x), CTRL, 0xf, 0xf, true));
}
constexpr int XOR1 = 0xB1, XOR2 = 0x4E, XOR7 = 0x141, XOR8 = 0x128, BCAST = 0x150;

__device__ __forceinline__ float xrow_sum(float x) {
  auto s = __builtin_amdgcn_permlane16_swap(__float_as_uint(x), __float_as_uint(x), false, false);
  x = __uint_as_float(s[0]) + __uint_as_float(s[1]);
  auto t = __builtin_amdgcn_permlane32_swap(__float_as_uint(x), __float_as_uint(x), false, false);
  return __uint_as_float(t[0]) + __uint_as_float(t[1]);
}
__device__ __forceinline__ float xrow_max(float x) {
  auto s = __builtin_amdgcn_permlane16_swap(__float_as_uint(x), __float_as_uint(x), false, false);
  x = fmaxf(__uint_as_float(s[0]), __uint_as_float(s[1]));
  auto t = __builtin_amdgcn_permlane32_swap(__float_as_uint(x), __float_as_uint(x), false, false);
  return fmaxf(__uint_as_float(t[0]), __uint_as_float(t[1]));
}
__device__ __forceinline__ float row_sum(float x) {
  x += dpp<XOR1>(x); x += dpp<XOR2>(x); x += dpp<XOR7>(x); x += dpp<XOR8>(x); return x;
}
__device__ __forceinline__ float row_max(float x) {
  x = fmaxf(x, dpp<XOR1>(x)); x = fmaxf(x, dpp<XOR2>(x)); x = fmaxf(x, dpp<XOR7>(x)); x = fmaxf(x, dpp<XOR8>(x)); return x;
}
__device__ __forceinline__ float tr_reduce16(float (&x)[16], int c) {
#define TR_STEP(HALF, CTRL, BIT)                                                     \
  _Pragma("unroll") for (int h = 0; h < (HALF); ++h) {                               \
    const float lo_ = x[h], hi_ = x[h + (HALF)];                                     \
    const bool up_ = (c & (BIT)) != 0;                                               \
    const float send_ = up_ ? lo_ : hi_, keep_ = up_ ? hi_ : lo_;                    \
    x[h] = keep_ + dpp<CTRL>(send_);                                                 \
  }
  TR_STEP(8, XOR8, 8) TR_STEP(4, XOR7, 4) TR_STEP(2, XOR2, 2) TR_STEP(1, XOR1, 1)
#undef TR_STEP
  return x[0];
}

#define SCHED_FENCE() __builtin_amdgcn_sched_barrier(0)
#define STAMP(i) do {} while (0)

#define KV_NT 1
__device__ __forceinline__ u32x4 ld_nt(const u16* ub, u32 off) {
  return __builtin_nontemporal_load(reinterpret_cast<const u32x4*>(reinterpret_cast<const char*>(ub) + off));
}
#define W_NT 0
#define KV_PAT 1
__device__ __forceinline__ u32x4 ld16(const u16* ub, u32 off) {
  return *reinterpret_cast<const u32x4*>(reinterpret_cast<const char*>(ub) + off);
}
__device__ __forceinline__ u32x2 ld8(const u16* ub, u32 off) {
  return *reinterpret_cast<const u32x2*>(reinterpret_cast<const char*>(ub) + off);
}
__device__ __forceinline__ u32 ld2(const u16* ub, u32 off) {
  return *reinterpret_cast<const u16*>(reinterpret_cast<const char*>(ub) + off);
}

constexpr int L_QP = 0;
constexpr int L_F1 = L_QP + NWAVE * DH;
constexpr int L_OP = L_F1 + NWAVE * 16;
constexpr int L_ML = L_OP + NWAVE * DH;
constexpr int L_RED = L_ML + NWAVE * 2;
constexpr int L_END = L_RED + NWAVE * DM;

__global__ void __launch_bounds__(512, 2)
k_main(const u16* __restrict__ x, const u16* __restrict__ kc, const u16* __restrict__ vc, const u16* __restrict__ wq,
       const u16* __restrict__ wo, const u16* __restrict__ w1, const u16* __restrict__ w2, float* __restrict__ ws,
       const u16* __restrict__ gw, const u16* __restrict__ outp) {
  __shared__ __attribute__((aligned(16))) float lds[L_END];
  const int bid = blockIdx.x, xcd = bid & 7, idx = bid >> 3;
  const int h = xcd * 4 + (idx >> 3), b = (idx >> 2) & 1, s = idx & 3;
  const int tid = threadIdx.x, lane = tid & 63, wave = __builtin_amdgcn_readfirstlane(tid >> 6);
  const int r = lane >> 4, c = lane & 15;
  const bool ffn_on = idx < 27;
  const int fg = xcd * 27 + (ffn_on ? idx : 26);

  STAMP(0);
  u32x2 xq[5];
#pragma unroll
  for (int j = 0; j < 5; ++j) xq[j] = ld8(x + b * DM + wave * 80 + 4 * j, r * 40);
  SCHED_FENCE();
  u32x4 Wq[20], KV[64], Wo[20];
  constexpr int KVSTR = 16 * DH;
  const size_t kvbase = ((size_t)(b * NH + h) * SK + s * TOKWG + (wave >> 2) * (4 * TOKW) + (wave & 3) * 4) * DH;
  const u16* kp = kc + kvbase;
  const u16* vp = vc + kvbase;
  const u32 loff = lane * 16;
  {
    const u32 off = (u32)(r * 20) * (NH * DH * 2) + 16 * c;
#pragma unroll
    for (int j = 0; j < 20; ++j) {
      Wq[j] = ld16(wq + (size_t)(wave * 80 + j) * (NH * DH) + h * DH, off);
      SCHED_FENCE();
      KV[j] = ld_nt(kp + j * KVSTR, loff);
      SCHED_FENCE();
    }
  }
#pragma unroll
  for (int i = 20; i < 24; ++i) { KV[i] = ld_nt(kp + i * KVSTR, loff); SCHED_FENCE(); }
  const u16* wop = wo + (size_t)(h * DH + wave * 16) * DM;
  const u32 wooff = (u32)r * (4 * DM * 2) + 16 * c;
#define PRE 5
#define ISSUE_T(T)                                                                                  \
  do {                                                                                              \
    if ((T) >= 0 && (T) < 20) {                                                                     \
      SCHED_FENCE();                                                                                \
      Wo[T] = ld16(wop + (2 * (((T) >> 1) / 5) + ((T) & 1)) * DM + (((T) >> 1) % 5) * 128, wooff);  \
      SCHED_FENCE();                                                                                \
      _Pragma("unroll") for (int t_ = 0; t_ < 2; ++t_) {                                            \
        const int kv_ = 24 + 2 * (T) + t_;                                                          \
        KV[kv_] = ld_nt((kv_ < 32 ? kp + kv_ * KVSTR : vp + (kv_ - 32) * KVSTR), loff);             \
        SCHED_FENCE();                                                                              \
      }                                                                                             \
    }                                                                                               \
  } while (0)
#define PERM_Q(Q)                                                                                   \
  do {                                                                                              \
    _Pragma("unroll") for (int k_ = 0; k_ < 4; ++k_) {                                              \
      const u32 a_ = Wo[2 * (Q)][k_], b_ = Wo[2 * (Q) + 1][k_];                                     \
      Wo[2 * (Q)][k_] = __builtin_amdgcn_perm(b_, a_, 0x05040100u);                                 \
      Wo[2 * (Q) + 1][k_] = __builtin_amdgcn_perm(b_, a_, 0x07060302u);                             \
    }                                                                                               \
  } while (0)
#define AFTER_KV(I)                                                                                 \
  do {                                                                                              \
    if ((I) % 3 == 2) ISSUE_T(PRE + (I) / 3);                                                       \
    if ((I) >= 26 && ((I) - 26) % 4 == 0) PERM_Q(((I) - 26) / 4);                                   \
  } while (0)

#define ABAR 0
  STAMP(1);
  {
    float qa[8];
#pragma unroll
    for (int k = 0; k < 8; ++k) qa[k] = 0.f;
#pragma unroll
    for (int j = 0; j < 20; ++j) {
      const u32 xw = xq[j >> 2][(j >> 1) & 1];
      const float xv = (j & 1) ? bfhi(xw) : bflo(xw);
#pragma unroll
      for (int k = 0; k < 4; ++k) {
        qa[2 * k] = fmaf(xv, bflo(Wq[j][k]), qa[2 * k]);
        qa[2 * k + 1] = fmaf(xv, bfhi(Wq[j][k]), qa[2 * k + 1]);
      }
    }
#pragma unroll
    for (int k = 0; k < 8; ++k) qa[k] = xrow_sum(qa[k]);
    if (r == 0) {
      *reinterpret_cast<f32x4*>(&lds[L_QP + wave * DH + 8 * c]) = f32x4{qa[0], qa[1], qa[2], qa[3]};
      *reinterpret_cast<f32x4*>(&lds[L_QP + wave * DH + 8 * c + 4]) = f32x4{qa[4], qa[5], qa[6], qa[7]};
    }
  }
  SCHED_FENCE();
  STAMP(2);
  const u32x4 w1a = ld16(w1 + (size_t)(wave * 80) * FF + fg * 8, (u32)lane * (FF * 2));
  const u32x4 w1b = ld16(w1 + (size_t)(wave * 80 + 64) * FF + fg * 8, (u32)c * (FF * 2));
  const u32 xa0 = ld2(x + wave * 80, 2 * lane), xa1 = ld2(x + DM + wave * 80, 2 * lane);
  const u32 xb0 = ld2(x + wave * 80 + 64, 2 * c), xb1 = ld2(x + DM + wave * 80 + 64, 2 * c);
  SCHED_FENCE();
#pragma unroll
  for (int T = 0; T < PRE; ++T) ISSUE_T(T);
  __syncthreads();
  STAMP(3);
  u32 qpk[4];
  {
    f32x4 q0 = {0.f, 0.f, 0.f, 0.f}, q1 = {0.f, 0.f, 0.f, 0.f};
#pragma unroll
    for (int w = 0; w < NWAVE; ++w) {
      q0 += *reinterpret_cast<const f32x4*>(&lds[L_QP + w * DH + 8 * c]);
      q1 += *reinterpret_cast<const f32x4*>(&lds[L_QP + w * DH + 8 * c + 4]);
    }
    qpk[0] = pk_bf(q0[0], q0[1]); qpk[1] = pk_bf(q0[2], q0[3]);
    qpk[2] = pk_bf(q1[0], q1[1]); qpk[3] = pk_bf(q1[2], q1[3]);
  }

  float sc[2];
  u32x2 w2r[8];
#pragma unroll
  for (int g = 0; g < 2; ++g) {
    float xx[16];
#pragma unroll
    for (int i = 0; i < 16; ++i) {
      const u32x4 kk = KV[g * 16 + i];
      float d = dot2(kk[0], qpk[0], 0.f);
      d = dot2(kk[1], qpk[1], d);
      d = dot2(kk[2], qpk[2], d);
      xx[i] = dot2(kk[3], qpk[3], d);
      AFTER_KV(g * 16 + i);
    }
    STAMP(4 + g);
    const float t = tr_reduce16(xx, c);
    sc[g] = rbf(rbf(t) * SCALE);
    if (g == 0) {
      SCHED_FENCE();
      float fa[16];
      const float a0 = bflo(xa0), a1 = bflo(xa1);
      const float b0 = lane < 16 ? bflo(xb0) : 0.f, b1 = lane < 16 ? bflo(xb1) : 0.f;
#pragma unroll
      for (int k = 0; k < 4; ++k) {
        fa[2 * k] = fmaf(a0, bflo(w1a[k]), b0 * bflo(w1b[k]));
        fa[2 * k + 1] = fmaf(a0, bfhi(w1a[k]), b0 * bfhi(w1b[k]));
        fa[8 + 2 * k] = fmaf(a1, bflo(w1a[k]), b1 * bflo(w1b[k]));
        fa[8 + 2 * k + 1] = fmaf(a1, bfhi(w1a[k]), b1 * bfhi(w1b[k]));
      }
      float ft = tr_reduce16(fa, c);
      ft = xrow_sum(ft);
      if (r == 0) lds[L_F1 + wave * 16 + c] = ft;
      SCHED_FENCE();
      const u32 col = (tid < 160 ? tid : 0) * 8;
#pragma unroll
      for (int j = 0; j < 8; ++j) w2r[j] = ld8(w2 + (size_t)(fg * 8 + j) * DM, col);
      SCHED_FENCE();
    }
  }
  {
    const float mw = xrow_max(row_max(fmaxf(sc[0], sc[1])));
    if (lane == 0) lds[L_ML + wave] = mw;
  }
  __syncthreads();
  float m2;
  {
    const f32x4 t0 = *reinterpret_cast<const f32x4*>(&lds[L_ML]), t1 = *reinterpret_cast<const f32x4*>(&lds[L_ML + 4]);
    m2 = fmaxf(fmaxf(fmaxf(t0[0], t0[1]), fmaxf(t0[2], t0[3])), fmaxf(fmaxf(t1[0], t1[1]), fmaxf(t1[2], t1[3]))) * LOG2E;
  }
  float p[2];
  p[0] = __builtin_amdgcn_exp2f(fmaf(sc[0], LOG2E, -m2));
  p[1] = __builtin_amdgcn_exp2f(fmaf(sc[1], LOG2E, -m2));
  const float lsum = xrow_sum(row_sum(p[0] + p[1]));
  if (lane == 0) lds[L_ML + 8 + wave] = lsum;

  {
    float hval;
    float t = 0.f;
#pragma unroll
    for (int w = 0; w < NWAVE; ++w) t += lds[L_F1 + w * 16 + c];
    t = rbf(t);
    hval = rbf(t / (1.f + __expf(-t)));
    float f0[4] = {0.f, 0.f, 0.f, 0.f}, f1[4] = {0.f, 0.f, 0.f, 0.f};
#define FFN2_STEP(J)                                                                  \
    {                                                                                 \
      const float h0 = dpp<BCAST + (J)>(hval), h1 = dpp<BCAST + 8 + (J)>(hval);       \
      const float w0 = bflo(w2r[J][0]), w1_ = bfhi(w2r[J][0]), w2_ = bflo(w2r[J][1]), w3 = bfhi(w2r[J][1]); \
      f0[0] = fmaf(h0, w0, f0[0]); f0[1] = fmaf(h0, w1_, f0[1]); f0[2] = fmaf(h0, w2_, f0[2]); f0[3] = fmaf(h0, w3, f0[3]); \
      f1[0] = fmaf(h1, w0, f1[0]); f1[1] = fmaf(h1, w1_, f1[1]); f1[2] = fmaf(h1, w2_, f1[2]); f1[3] = fmaf(h1, w3, f1[3]); \
    }
    FFN2_STEP(0) FFN2_STEP(1) FFN2_STEP(2) FFN2_STEP(3) FFN2_STEP(4) FFN2_STEP(5) FFN2_STEP(6) FFN2_STEP(7)
#undef FFN2_STEP
    asm volatile("" : "+v"(f0[0]), "+v"(f0[1]), "+v"(f0[2]), "+v"(f0[3]), "+v"(f1[0]), "+v"(f1[1]), "+v"(f1[2]), "+v"(f1[3]));
    if (ffn_on && tid < 160) {
      float* fp = ws + WS_F + (size_t)fg * NB * DM + tid * 4;
      *reinterpret_cast<f32x4*>(fp) = f32x4{f0[0], f0[1], f0[2], f0[3]};
      *reinterpret_cast<f32x4*>(fp + DM) = f32x4{f1[0], f1[1], f1[2], f1[3]};
    }
  }
  SCHED_FENCE();

  float acc[8];
#pragma unroll
  for (int k = 0; k < 8; ++k) acc[k] = 0.f;
#define PV_STEP(I)                                                                    \
  {                                                                                   \
    const float pb = dpp<BCAST + ((I) & 15)>(p[(I) >> 4]);                            \
    const u32x4 vv = KV[32 + (I)];                                                    \
    _Pragma("unroll") for (int k = 0; k < 4; ++k) {                                   \
      acc[2 * k] = fmaf(pb, bflo(vv[k]), acc[2 * k]);                                 \
      acc[2 * k + 1] = fmaf(pb, bfhi(vv[k]), acc[2 * k + 1]);                         \
    }                                                                                 \
    AFTER_KV(32 + (I));                                                               \
  }
  PV_STEP(0) PV_STEP(1) PV_STEP(2) PV_STEP(3) PV_STEP(4) PV_STEP(5) PV_STEP(6) PV_STEP(7)
  PV_STEP(8) PV_STEP(9) PV_STEP(10) PV_STEP(11) PV_STEP(12) PV_STEP(13) PV_STEP(14) PV_STEP(15)
  PV_STEP(16) PV_STEP(17) PV_STEP(18) PV_STEP(19) PV_STEP(20) PV_STEP(21) PV_STEP(22) PV_STEP(23)
  PV_STEP(24) PV_STEP(25) PV_STEP(26) PV_STEP(27) PV_STEP(28) PV_STEP(29) PV_STEP(30) PV_STEP(31)
#undef PV_STEP
  STAMP(6);
#pragma unroll
  for (int k = 0; k < 8; ++k) acc[k] = xrow_sum(acc[k]);
  if (r == 0) {
    *reinterpret_cast<f32x4*>(&lds[L_OP + wave * DH + 8 * c]) = f32x4{acc[0], acc[1], acc[2], acc[3]};
    *reinterpret_cast<f32x4*>(&lds[L_OP + wave * DH + 8 * c + 4]) = f32x4{acc[4], acc[5], acc[6], acc[7]};
  }
  __syncthreads();

  STAMP(7);
  {
    f32x4 o4 = {0.f, 0.f, 0.f, 0.f};
#pragma unroll
    for (int w = 0; w < 8; ++w) o4 += *reinterpret_cast<const f32x4*>(&lds[L_OP + w * DH + (wave * 4 + r) * 4]);
    if (tid == 0) {
      const f32x4 t0 = *reinterpret_cast<const f32x4*>(&lds[L_ML + 8]), t1 = *reinterpret_cast<const f32x4*>(&lds[L_ML + 12]);
      const float L = ((t0[0] + t0[1]) + (t0[2] + t0[3])) + ((t1[0] + t1[1]) + (t1[2] + t1[3]));
      *reinterpret_cast<f32x2*>(ws + WS_ML + (size_t)((b * NH + h) * NSPLIT + s) * 2) = f32x2{m2, L};
    }
    const u32 o01 = pk_bf(o4[0], o4[1]), o23 = pk_bf(o4[2], o4[3]);
    float y[40];
#pragma unroll
    for (int seg = 0; seg < 5; ++seg)
#pragma unroll
      for (int k = 0; k < 4; ++k) {
        y[(2 * k) * 5 + seg] = dot2(Wo[2 * (5 + seg)][k], o23, dot2(Wo[2 * seg][k], o01, 0.f));
        y[(2 * k + 1) * 5 + seg] = dot2(Wo[2 * (5 + seg) + 1][k], o23, dot2(Wo[2 * seg + 1][k], o01, 0.f));
      }
    float z[20];
#pragma unroll
    for (int v = 0; v < 20; ++v) {
      auto t = __builtin_amdgcn_permlane32_swap(__float_as_uint(y[v]), __float_as_uint(y[v + 20]), false, false);
      z[v] = __uint_as_float(t[0]) + __uint_as_float(t[1]);
    }
    float u[10];
#pragma unroll
    for (int v = 0; v < 10; ++v) {
      auto t = __builtin_amdgcn_permlane16_swap(__float_as_uint(z[v]), __float_as_uint(z[v + 10]), false, false);
      u[v] = __uint_as_float(t[0]) + __uint_as_float(t[1]);
    }
#pragma unroll
    for (int seg = 0; seg < 5; ++seg)
      *reinterpret_cast<f32x2*>(&lds[L_RED + wave * DM + seg * 128 + 8 * c + 2 * r]) = f32x2{u[seg], u[5 + seg]};
  }
  __syncthreads();
  if (tid < 160) {
    f32x4 t = {0.f, 0.f, 0.f, 0.f};
#pragma unroll
    for (int w = 0; w < NWAVE; ++w) t += *reinterpret_cast<const f32x4*>(&lds[L_RED + w * DM + tid * 4]);
    *reinterpret_cast<f32x4*>(ws + WS_P + (size_t)((b * NH + h) * NSPLIT + s) * DM + tid * 4) = t;
  }
}

__global__ void __launch_bounds__(256)
k_fin(const u16* __restrict__ x, const u16* __restrict__ gw, const float* __restrict__ ws, u16* __restrict__ out) {
  __shared__ __attribute__((aligned(16))) float red[4][2][32];
  const int b = blockIdx.x / 20, cb = blockIdx.x % 20;
  const int tid = threadIdx.x, lane = tid & 63, wave = tid >> 6;
  const int c4 = tid & 7, rg = tid >> 3;
  const int col = cb * 32 + c4 * 4;
  f32x4 pr[4];
#pragma unroll
  for (int s = 0; s < 4; ++s)
    pr[s] = *reinterpret_cast<const f32x4*>(ws + WS_P + (size_t)((b * NH + rg) * NSPLIT + s) * DM + col);
  const f32x4 ml0 = *reinterpret_cast<const f32x4*>(ws + WS_ML + (size_t)(b * NH + rg) * NSPLIT * 2);
  const f32x4 ml1 = *reinterpret_cast<const f32x4*>(ws + WS_ML + (size_t)(b * NH + rg) * NSPLIT * 2 + 4);
  f32x4 fr[7];
#pragma unroll
  for (int j = 0; j < 7; ++j) {
    const int f = rg + 32 * j;
    const int fc = f < NFG ? f : NFG - 1;
    fr[j] = *reinterpret_cast<const f32x4*>(ws + WS_F + (size_t)(fc * NB + b) * DM + col);
  }
  u32 xg[5], gg[5];
#pragma unroll
  for (int j = 0; j < 5; ++j) {
    xg[j] = *reinterpret_cast<const u32*>(x + b * DM + 2 * (lane + 64 * j));
    gg[j] = *reinterpret_cast<const u32*>(gw + 2 * (lane + 64 * j));
  }
  float gs = 0.f;
#pragma unroll
  for (int j = 0; j < 5; ++j) gs += rbf(bflo(xg[j]) * bflo(gg[j])) + rbf(bfhi(xg[j]) * bfhi(gg[j]));
  gs = xrow_sum(row_sum(gs));
  gs = rbf(gs);
  const float g = rbf(1.f / (1.f + __expf(-gs)));
  const float M = fmaxf(fmaxf(ml0[0], ml0[2]), fmaxf(ml1[0], ml1[2]));
  const float e0 = __builtin_amdgcn_exp2f(ml0[0] - M), e1 = __builtin_amdgcn_exp2f(ml0[2] - M);
  const float e2 = __builtin_amdgcn_exp2f(ml1[0] - M), e3 = __builtin_amdgcn_exp2f(ml1[2] - M);
  const float L = e0 * ml0[1] + e1 * ml0[3] + e2 * ml1[1] + e3 * ml1[3];
  const float inv = 1.f / L;
  f32x4 a = (e0 * inv) * pr[0] + (e1 * inv) * pr[1] + (e2 * inv) * pr[2] + (e3 * inv) * pr[3];
  f32x4 f = fr[0] + fr[1] + fr[2] + fr[3] + fr[4] + fr[5];
  if (rg + 32 * 6 < NFG) f += fr[6];
#pragma unroll
  for (int k = 0; k < 4; ++k) {
    a[k] += dpp<XOR8>(a[k]); a[k] = xrow_sum(a[k]);
    f[k] += dpp<XOR8>(f[k]); f[k] = xrow_sum(f[k]);
  }
  if (lane < 8) {
    *reinterpret_cast<f32x4*>(&red[wave][0][c4 * 4]) = a;
    *reinterpret_cast<f32x4*>(&red[wave][1][c4 * 4]) = f;
  }
  __syncthreads();
  if (tid < 32) {
    const float av = rbf(red[0][0][tid] + red[1][0][tid] + red[2][0][tid] + red[3][0][tid]);
    const float fv = rbf(red[0][1][tid] + red[1][1][tid] + red[2][1][tid] + red[3][1][tid]);
    const float t1 = rbf(g * av), t2 = rbf(rbf(1.f - g) * fv);
    out[b * DM + cb * 32 + tid] = __builtin_bit_cast(u16, __float2bfloat16(t1 + t2));
  }
}

}

extern "C" void kernel_launch(void* const* d_in, const int* in_sizes, int n_in, void* d_out, int out_size, void* d_ws,
                              size_t ws_size, hipStream_t stream) {
  const u16* x = (const u16*)d_in[0];
  const u16* kc = (const u16*)d_in[1];
  const u16* vc = (const u16*)d_in[2];
  const u16* wq = (const u16*)d_in[3];
  const u16* wo = (const u16*)d_in[4];
  const u16* w1 = (const u16*)d_in[6];
  const u16* w2 = (const u16*)d_in[7];
  const u16* gw = (const u16*)d_in[8];
  float* ws = (float*)d_ws;
  k_main<<<dim3(NB * NH * NSPLIT), dim3(512), 0, stream>>>(x, kc, vc, wq, wo, w1, w2, ws, gw, (const u16*)d_out);
  k_fin<<<dim3(NB * 20), dim3(256), 0, stream>>>(x, gw, ws, (u16*)d_out);
}
```

```cpp
#include <hip/hip_runtime.h>
#include <hip/hip_bf16.h>
#include <stdint.h>

typedef unsigned int u32;
typedef unsigned short u16;
typedef __attribute__((ext_vector_type(4))) unsigned u32x4;
typedef __attribute__((ext_vector_type(2))) unsigned u32x2;
typedef __attribute__((ext_vector_type(4))) float f32x4;
typedef __attribute__((ext_vector_type(2))) float f32x2;
typedef __attribute__((ext_vector_type(2))) __bf16 bf16x2;

namespace {

constexpr int NB = 2, DM = 640, NH = 32, SK = 4096, DH = 128, FF = 1728;
constexpr int NSPLIT = 4, TOKWG = SK / NSPLIT, NWAVE = 8, TOKW = TOKWG / NWAVE;
constexpr int NFG = FF / 8;
constexpr float LOG2E = 1.4426950408889634f;
constexpr float SCALE = 0.088388347648318440f;

constexpr size_t WS_EPOCH = 0, WS_FLAG = 64;
constexpr size_t WS_P = WS_FLAG + 512;
constexpr size_t WS_ML = WS_P + (size_t)NB * NH * NSPLIT * DM;
constexpr size_t WS_F = WS_ML + (size_t)NB * NH * NSPLIT * 2;
constexpr size_t WS_END = WS_F + (size_t)NFG * NB * DM;
typedef __attribute__((address_space(1))) unsigned long long gu64;
#define RLX_AGENT __ATOMIC_RELAXED, __HIP_MEMORY_SCOPE_AGENT
constexpr int SC1 = 16;

__device__ __forceinline__ float dot2(u32 a, u32 b, float acc) {
  return __builtin_amdgcn_fdot2_f32_bf16(__builtin_bit_cast(bf16x2, a), __builtin_bit_cast(bf16x2, b), acc, false);
}
__device__ __forceinline__ float bflo(u32 v) { return __builtin_bit_cast(float, v << 16); }
__device__ __forceinline__ float bfhi(u32 v) { return __builtin_bit_cast(float, v & 0xffff0000u); }
__device__ __forceinline__ float rbf(float x) { return __bfloat162float(__float2bfloat16(x)); }
__device__ __forceinline__ u32 pk_bf(float lo, float hi) {
  const u32 a = __builtin_bit_cast(u16, __float2bfloat16(lo));
  const u32 b = __builtin_bit_cast(u16, __float2bfloat16(hi));
  return a | (b << 16);
}
template <int CTRL> __device__ __forceinline__ float dpp(float x) {
  return __builtin_bit_cast(float, __builtin_amdgcn_mov_dpp(__builtin_bit_cast(int, x), CTRL, 0xf, 0xf, true));
}
constexpr int XOR1 = 0xB1, XOR2 = 0x4E, XOR7 = 0x141, XOR8 = 0x128, BCAST = 0x150;

__device__ __forceinline__ float xrow_sum(float x) {
  auto s = __builtin_amdgcn_permlane16_swap(__float_as_uint(x), __float_as_uint(x), false, false);
  x = __uint_as_float(s[0]) + __uint_as_float(s[1]);
  auto t = __builtin_amdgcn_permlane32_swap(__float_as_uint(x), __float_as_uint(x), false, false);
  return __uint_as_float(t[0]) + __uint_as_float(t[1]);
}
__device__ __forceinline__ float xrow_max(float x) {
  auto s = __builtin_amdgcn_permlane16_swap(__float_as_uint(x), __float_as_uint(x), false, false);
  x = fmaxf(__uint_as_float(s[0]), __uint_as_float(s[1]));
  auto t = __builtin_amdgcn_permlane32_swap(__float_as_uint(x), __float_as_uint(x), false, false);
  return fmaxf(__uint_as_float(t[0]), __uint_as_float(t[1]));
}
__device__ __forceinline__ float row_sum(float x) {
  x += dpp<XOR1>(x); x += dpp<XOR2>(x); x += dpp<XOR7>(x); x += dpp<XOR8>(x); return x;
}
__device__ __forceinline__ float row_max(float x) {
  x = fmaxf(x, dpp<XOR1>(x)); x = fmaxf(x, dpp<XOR2>(x)); x = fmaxf(x, dpp<XOR7>(x)); x = fmaxf(x, dpp<XOR8>(x)); return x;
}
__device__ __forceinline__ float tr_reduce16(float (&x)[16], int c) {
#define TR_STEP(HALF, CTRL, BIT)                                                     \
  _Pragma("unroll") for (int h = 0; h < (HALF); ++h) {                               \
    const float lo_ = x[h], hi_ = x[h + (HALF)];                                     \
    const bool up_ = (c & (BIT)) != 0;                                               \
    const float send_ = up_ ? lo_ : hi_, keep_ = up_ ? hi_ : lo_;                    \
    x[h] = keep_ + dpp<CTRL>(send_);                                                 \
  }
  TR_STEP(8, XOR8, 8) TR_STEP(4, XOR7, 4) TR_STEP(2, XOR2, 2) TR_STEP(1, XOR1, 1)
#undef TR_STEP
  return x[0];
}

#define SCHED_FENCE() __builtin_amdgcn_sched_barrier(0)
#define STAMP(i) do {} while (0)

#define KV_NT 1
__device__ __forceinline__ u32x4 ld_nt(const u16* ub, u32 off) {
  return __builtin_nontemporal_load(reinterpret_cast<const u32x4*>(reinterpret_cast<const char*>(ub) + off));
}
#define W_NT 0
#define KV_PAT 1
__device__ __forceinline__ u32x4 ld16(const u16* ub, u32 off) {
  return *reinterpret_cast<const u32x4*>(reinterpret_cast<const char*>(ub) + off);
}
__device__ __forceinline__ u32x2 ld8(const u16* ub, u32 off) {
  return *reinterpret_cast<const u32x2*>(reinterpret_cast<const char*>(ub) + off);
}
__device__ __forceinline__ u32 ld2(const u16* ub, u32 off) {
  return *reinterpret_cast<const u16*>(reinterpret_cast<const char*>(ub) + off);
}

constexpr int L_QP = 0;
constexpr int L_F1 = L_QP + NWAVE * DH;
constexpr int L_OP = L_F1 + NWAVE * 16;
constexpr int L_ML = L_OP + NWAVE * DH;
constexpr int L_RED = L_ML + NWAVE * 2;
constexpr int L_END = L_RED + NWAVE * DM;

__global__ void __launch_bounds__(512, 2)
k_main(const u16* __restrict__ x, const u16* __restrict__ kc, const u16* __restrict__ vc, const u16* __restrict__ wq,
       const u16* __restrict__ wo, const u16* __restrict__ w1, const u16* __restrict__ w2, float* __restrict__ ws,
       const u16* __restrict__ gw, u16* __restrict__ outp) {
  __shared__ __attribute__((aligned(16))) float lds[L_END];
  const int bid = blockIdx.x, xcd = bid & 7, idx = bid >> 3;
  const int h = xcd * 4 + (idx >> 3), b = (idx >> 2) & 1, s = idx & 3;
  const int tid = threadIdx.x, lane = tid & 63, wave = __builtin_amdgcn_readfirstlane(tid >> 6);
  const int r = lane >> 4, c = lane & 15;
  const bool ffn_on = idx < 27;
  const int fg = xcd * 27 + (ffn_on ? idx : 26);

  STAMP(0);
  const __amdgpu_buffer_rsrc_t wsr = __builtin_amdgcn_make_buffer_rsrc(ws, 0, (int)(WS_END * 4), 0x00020000);
  const unsigned long long epoch = __hip_atomic_load((gu64*)(ws + WS_EPOCH), RLX_AGENT);
  SCHED_FENCE();
  u32x2 xq[5];
#pragma unroll
  for (int j = 0; j < 5; ++j) xq[j] = ld8(x + b * DM + wave * 80 + 4 * j, r * 40);
  SCHED_FENCE();
  u32x4 Wq[20], KV[64], Wo[20];
  constexpr int KVSTR = 16 * DH;
  const size_t kvbase = ((size_t)(b * NH + h) * SK + s * TOKWG + (wave >> 2) * (4 * TOKW) + (wave & 3) * 4) * DH;
  const u16* kp = kc + kvbase;
  const u16* vp = vc + kvbase;
  const u32 loff = lane * 16;
  {
    const u32 off = (u32)(r * 20) * (NH * DH * 2) + 16 * c;
#pragma unroll
    for (int j = 0; j < 20; ++j) {
      Wq[j] = ld16(wq + (size_t)(wave * 80 + j) * (NH * DH) + h * DH, off);
      SCHED_FENCE();
      KV[j] = ld_nt(kp + j * KVSTR, loff);
      SCHED_FENCE();
    }
  }
#pragma unroll
  for (int i = 20; i < 24; ++i) { KV[i] = ld_nt(kp + i * KVSTR, loff); SCHED_FENCE(); }
  const u16* wop = wo + (size_t)(h * DH + wave * 16) * DM;
  const u32 wooff = (u32)r * (4 * DM * 2) + 16 * c;
#define PRE 5
#define ISSUE_T(T)                                                                                  \
  do {                                                                                              \
    if ((T) >= 0 && (T) < 20) {                                                                     \
      SCHED_FENCE();                                                                                \
      Wo[T] = ld16(wop + (2 * (((T) >> 1) / 5) + ((T) & 1)) * DM + (((T) >> 1) % 5) * 128, wooff);  \
      SCHED_FENCE();                                                                                \
      _Pragma("unroll") for (int t_ = 0; t_ < 2; ++t_) {                                            \
        const int kv_ = 24 + 2 * (T) + t_;                                                          \
        KV[kv_] = ld_nt((kv_ < 32 ? kp + kv_ * KVSTR : vp + (kv_ - 32) * KVSTR), loff);             \
        SCHED_FENCE();                                                                              \
      }                                                                                             \
    }                                                                                               \
  } while (0)
#define PERM_Q(Q)                                                                                   \
  do {                                                                                              \
    _Pragma("unroll") for (int k_ = 0; k_ < 4; ++k_) {                                              \
      const u32 a_ = Wo[2 * (Q)][k_], b_ = Wo[2 * (Q) + 1][k_];                                     \
      Wo[2 * (Q)][k_] = __builtin_amdgcn_perm(b_, a_, 0x05040100u);                                 \
      Wo[2 * (Q) + 1][k_] = __builtin_amdgcn_perm(b_, a_, 0x07060302u);                             \
    }                                                                                               \
  } while (0)
#define AFTER_KV(I)                                                                                 \
  do {                                                                                              \
    if ((I) % 3 == 2) ISSUE_T(PRE + (I) / 3);                                                       \
    if ((I) >= 26 && ((I) - 26) % 4 == 0) PERM_Q(((I) - 26) / 4);                                   \
  } while (0)

#define ABAR 0
  STAMP(1);
  {
    float qa[8];
#pragma unroll
    for (int k = 0; k < 8; ++k) qa[k] = 0.f;
#pragma unroll
    for (int j = 0; j < 20; ++j) {
      const u32 xw = xq[j >> 2][(j >> 1) & 1];
      const float xv = (j & 1) ? bfhi(xw) : bflo(xw);
#pragma unroll
      for (int k = 0; k < 4; ++k) {
        qa[2 * k] = fmaf(xv, bflo(Wq[j][k]), qa[2 * k]);
        qa[2 * k + 1] = fmaf(xv, bfhi(Wq[j][k]), qa[2 * k + 1]);
      }
    }
#pragma unroll
    for (int k = 0; k < 8; ++k) qa[k] = xrow_sum(qa[k]);
    if (r == 0) {
      *reinterpret_cast<f32x4*>(&lds[L_QP + wave * DH + 8 * c]) = f32x4{qa[0], qa[1], qa[2], qa[3]};
      *reinterpret_cast<f32x4*>(&lds[L_QP + wave * DH + 8 * c + 4]) = f32x4{qa[4], qa[5], qa[6], qa[7]};
    }
  }
  SCHED_FENCE();
  STAMP(2);
  const u32x4 w1a = ld16(w1 + (size_t)(wave * 80) * FF + fg * 8, (u32)lane * (FF * 2));
  const u32x4 w1b = ld16(w1 + (size_t)(wave * 80 + 64) * FF + fg * 8, (u32)c * (FF * 2));
  const u32 xa0 = ld2(x + wave * 80, 2 * lane), xa1 = ld2(x + DM + wave * 80, 2 * lane);
  const u32 xb0 = ld2(x + wave * 80 + 64, 2 * c), xb1 = ld2(x + DM + wave * 80 + 64, 2 * c);
  SCHED_FENCE();
#pragma unroll
  for (int T = 0; T < PRE; ++T) ISSUE_T(T);
  __syncthreads();
  STAMP(3);
  u32 qpk[4];
  {
    f32x4 q0 = {0.f, 0.f, 0.f, 0.f}, q1 = {0.f, 0.f, 0.f, 0.f};
#pragma unroll
    for (int w = 0; w < NWAVE; ++w) {
      q0 += *reinterpret_cast<const f32x4*>(&lds[L_QP + w * DH + 8 * c]);
      q1 += *reinterpret_cast<const f32x4*>(&lds[L_QP + w * DH + 8 * c + 4]);
    }
    qpk[0] = pk_bf(q0[0], q0[1]); qpk[1] = pk_bf(q0[2], q0[3]);
    qpk[2] = pk_bf(q1[0], q1[1]); qpk[3] = pk_bf(q1[2], q1[3]);
  }

  float sc[2];
  u32x2 w2r[8];
#pragma unroll
  for (int g = 0; g < 2; ++g) {
    float xx[16];
#pragma unroll
    for (int i = 0; i < 16; ++i) {
      const u32x4 kk = KV[g * 16 + i];
      float d = dot2(kk[0], qpk[0], 0.f);
      d = dot2(kk[1], qpk[1], d);
      d = dot2(kk[2], qpk[2], d);
      xx[i] = dot2(kk[3], qpk[3], d);
      AFTER_KV(g * 16 + i);
    }
    STAMP(4 + g);
    const float t = tr_reduce16(xx, c);
    sc[g] = rbf(rbf(t) * SCALE);
    if (g == 0) {
      SCHED_FENCE();
      float fa[16];
      const float a0 = bflo(xa0), a1 = bflo(xa1);
      const float b0 = lane < 16 ? bflo(xb0) : 0.f, b1 = lane < 16 ? bflo(xb1) : 0.f;
#pragma unroll
      for (int k = 0; k < 4; ++k) {
        fa[2 * k] = fmaf(a0, bflo(w1a[k]), b0 * bflo(w1b[k]));
        fa[2 * k + 1] = fmaf(a0, bfhi(w1a[k]), b0 * bfhi(w1b[k]));
        fa[8 + 2 * k] = fmaf(a1, bflo(w1a[k]), b1 * bflo(w1b[k]));
        fa[8 + 2 * k + 1] = fmaf(a1, bfhi(w1a[k]), b1 * bfhi(w1b[k]));
      }
      float ft = tr_reduce16(fa, c);
      ft = xrow_sum(ft);
      if (r == 0) lds[L_F1 + wave * 16 + c] = ft;
      SCHED_FENCE();
      const u32 col = (tid < 160 ? tid : 0) * 8;
#pragma unroll
      for (int j = 0; j < 8; ++j) w2r[j] = ld8(w2 + (size_t)(fg * 8 + j) * DM, col);
      SCHED_FENCE();
    }
  }
  {
    const float mw = xrow_max(row_max(fmaxf(sc[0], sc[1])));
    if (lane == 0) lds[L_ML + wave] = mw;
  }
  __syncthreads();
  float m2;
  {
    const f32x4 t0 = *reinterpret_cast<const f32x4*>(&lds[L_ML]), t1 = *reinterpret_cast<const f32x4*>(&lds[L_ML + 4]);
    m2 = fmaxf(fmaxf(fmaxf(t0[0], t0[1]), fmaxf(t0[2], t0[3])), fmaxf(fmaxf(t1[0], t1[1]), fmaxf(t1[2], t1[3]))) * LOG2E;
  }
  float p[2];
  p[0] = __builtin_amdgcn_exp2f(fmaf(sc[0], LOG2E, -m2));
  p[1] = __builtin_amdgcn_exp2f(fmaf(sc[1], LOG2E, -m2));
  const float lsum = xrow_sum(row_sum(p[0] + p[1]));
  if (lane == 0) lds[L_ML + 8 + wave] = lsum;

  {
    float hval;
    float t = 0.f;
#pragma unroll
    for (int w = 0; w < NWAVE; ++w) t += lds[L_F1 + w * 16 + c];
    t = rbf(t);
    hval = rbf(t / (1.f + __expf(-t)));
    float f0[4] = {0.f, 0.f, 0.f, 0.f}, f1[4] = {0.f, 0.f, 0.f, 0.f};
#define FFN2_STEP(J)                                                                  \
    {                                                                                 \
      const float h0 = dpp<BCAST + (J)>(hval), h1 = dpp<BCAST + 8 + (J)>(hval);       \
      const float w0 = bflo(w2r[J][0]), w1_ = bfhi(w2r[J][0]), w2_ = bflo(w2r[J][1]), w3 = bfhi(w2r[J][1]); \
      f0[0] = fmaf(h0, w0, f0[0]); f0[1] = fmaf(h0, w1_, f0[1]); f0[2] = fmaf(h0, w2_, f0[2]); f0[3] = fmaf(h0, w3, f0[3]); \
      f1[0] = fmaf(h1, w0, f1[0]); f1[1] = fmaf(h1, w1_, f1[1]); f1[2] = fmaf(h1, w2_, f1[2]); f1[3] = fmaf(h1, w3, f1[3]); \
    }
    FFN2_STEP(0) FFN2_STEP(1) FFN2_STEP(2) FFN2_STEP(3) FFN2_STEP(4) FFN2_STEP(5) FFN2_STEP(6) FFN2_STEP(7)
#undef FFN2_STEP
    asm volatile("" : "+v"(f0[0]), "+v"(f0[1]), "+v"(f0[2]), "+v"(f0[3]), "+v"(f1[0]), "+v"(f1[1]), "+v"(f1[2]), "+v"(f1[3]));
    if (ffn_on && tid < 160) {
      const u32 fo = (u32)(WS_F + (size_t)fg * NB * DM + tid * 4) * 4;
      __builtin_amdgcn_raw_buffer_store_b128(__builtin_bit_cast(u32x4, f32x4{f0[0], f0[1], f0[2], f0[3]}), wsr, fo, 0, SC1);
      __builtin_amdgcn_raw_buffer_store_b128(__builtin_bit_cast(u32x4, f32x4{f1[0], f1[1], f1[2], f1[3]}), wsr, fo + DM * 4, 0, SC1);
    }
  }
  SCHED_FENCE();

  float acc[8];
#pragma unroll
  for (int k = 0; k < 8; ++k) acc[k] = 0.f;
#define PV_STEP(I)                                                                    \
  {                                                                                   \
    const float pb = dpp<BCAST + ((I) & 15)>(p[(I) >> 4]);                            \
    const u32x4 vv = KV[32 + (I)];                                                    \
    _Pragma("unroll") for (int k = 0; k < 4; ++k) {                                   \
      acc[2 * k] = fmaf(pb, bflo(vv[k]), acc[2 * k]);                                 \
      acc[2 * k + 1] = fmaf(pb, bfhi(vv[k]), acc[2 * k + 1]);                         \
    }                                                                                 \
    AFTER_KV(32 + (I));                                                               \
  }
  PV_STEP(0) PV_STEP(1) PV_STEP(2) PV_STEP(3) PV_STEP(4) PV_STEP(5) PV_STEP(6) PV_STEP(7)
  PV_STEP(8) PV_STEP(9) PV_STEP(10) PV_STEP(11) PV_STEP(12) PV_STEP(13) PV_STEP(14) PV_STEP(15)
  PV_STEP(16) PV_STEP(17) PV_STEP(18) PV_STEP(19) PV_STEP(20) PV_STEP(21) PV_STEP(22) PV_STEP(23)
  PV_STEP(24) PV_STEP(25) PV_STEP(26) PV_STEP(27) PV_STEP(28) PV_STEP(29) PV_STEP(30) PV_STEP(31)
#undef PV_STEP
  STAMP(6);
#pragma unroll
  for (int k = 0; k < 8; ++k) acc[k] = xrow_sum(acc[k]);
  if (r == 0) {
    *reinterpret_cast<f32x4*>(&lds[L_OP + wave * DH + 8 * c]) = f32x4{acc[0], acc[1], acc[2], acc[3]};
    *reinterpret_cast<f32x4*>(&lds[L_OP + wave * DH + 8 * c + 4]) = f32x4{acc[4], acc[5], acc[6], acc[7]};
  }
  __syncthreads();

  STAMP(7);
  {
    f32x4 o4 = {0.f, 0.f, 0.f, 0.f};
#pragma unroll
    for (int w = 0; w < 8; ++w) o4 += *reinterpret_cast<const f32x4*>(&lds[L_OP + w * DH + (wave * 4 + r) * 4]);
    if (tid == 0) {
      const f32x4 t0 = *reinterpret_cast<const f32x4*>(&lds[L_ML + 8]), t1 = *reinterpret_cast<const f32x4*>(&lds[L_ML + 12]);
      const float L = ((t0[0] + t0[1]) + (t0[2] + t0[3])) + ((t1[0] + t1[1]) + (t1[2] + t1[3]));
      __builtin_amdgcn_raw_buffer_store_b64(__builtin_bit_cast(u32x2, f32x2{m2, L}), wsr, (u32)(WS_ML + (size_t)((b * NH + h) * NSPLIT + s) * 2) * 4, 0, SC1);
    }
    const u32 o01 = pk_bf(o4[0], o4[1]), o23 = pk_bf(o4[2], o4[3]);
    float y[40];
#pragma unroll
    for (int seg = 0; seg < 5; ++seg)
#pragma unroll
      for (int k = 0; k < 4; ++k) {
        y[(2 * k) * 5 + seg] = dot2(Wo[2 * (5 + seg)][k], o23, dot2(Wo[2 * seg][k], o01, 0.f));
        y[(2 * k + 1) * 5 + seg] = dot2(Wo[2 * (5 + seg) + 1][k], o23, dot2(Wo[2 * seg + 1][k], o01, 0.f));
      }
    float z[20];
#pragma unroll
    for (int v = 0; v < 20; ++v) {
      auto t = __builtin_amdgcn_permlane32_swap(__float_as_uint(y[v]), __float_as_uint(y[v + 20]), false, false);
      z[v] = __uint_as_float(t[0]) + __uint_as_float(t[1]);
    }
    float u[10];
#pragma unroll
    for (int v = 0; v < 10; ++v) {
      auto t = __builtin_amdgcn_permlane16_swap(__float_as_uint(z[v]), __float_as_uint(z[v + 10]), false, false);
      u[v] = __uint_as_float(t[0]) + __uint_as_float(t[1]);
    }
#pragma unroll
    for (int seg = 0; seg < 5; ++seg)
      *reinterpret_cast<f32x2*>(&lds[L_RED + wave * DM + seg * 128 + 8 * c + 2 * r]) = f32x2{u[seg], u[5 + seg]};
  }
  __syncthreads();
  if (tid < 160) {
    f32x4 t = {0.f, 0.f, 0.f, 0.f};
#pragma unroll
    for (int w = 0; w < NWAVE; ++w) t += *reinterpret_cast<const f32x4*>(&lds[L_RED + w * DM + tid * 4]);
    __builtin_amdgcn_raw_buffer_store_b128(__builtin_bit_cast(u32x4, t), wsr, (u32)(WS_P + (size_t)((b * NH + h) * NSPLIT + s) * DM + tid * 4) * 4, 0, SC1);
  }
  asm volatile("s_waitcnt vmcnt(0)" ::: "memory");
  __syncthreads();
  const unsigned long long tag = epoch + 1ull;
  if (tid == 0) __hip_atomic_store((gu64*)(ws + WS_FLAG) + bid, tag, RLX_AGENT);
  STAMP(8);

  if (bid < NB * 20) {
    const int rb = bid / 20, cb = bid % 20;
    u32 xg[5], gg[5];
#pragma unroll
    for (int j = 0; j < 5; ++j) {
      xg[j] = *reinterpret_cast<const u32*>(x + rb * DM + 2 * (lane + 64 * j));
      gg[j] = *reinterpret_cast<const u32*>(gw + 2 * (lane + 64 * j));
    }
    if (wave == 0) {
      gu64* fl = (gu64*)(ws + WS_FLAG) + 4 * lane;
      for (unsigned spins = 0; spins < (1u << 22); ++spins) {
        const unsigned long long f0 = __hip_atomic_load(fl, RLX_AGENT), f1 = __hip_atomic_load(fl + 1, RLX_AGENT);
        const unsigned long long f2 = __hip_atomic_load(fl + 2, RLX_AGENT), f3 = __hip_atomic_load(fl + 3, RLX_AGENT);
        if (__all(f0 == tag && f1 == tag && f2 == tag && f3 == tag)) break;
        __builtin_amdgcn_s_sleep(1);
      }
      if (bid == 0 && lane == 0) __hip_atomic_store((gu64*)(ws + WS_EPOCH), tag, RLX_AGENT);
    }
    asm volatile("" ::: "memory");
    __syncthreads();
    const int c4 = tid & 7, rg = tid >> 3;
    const int col = cb * 32 + c4 * 4;
    const int hh = rg >> 1, s0 = (rg & 1) * 2;
    const u32 po = (u32)(WS_P + (size_t)((rb * NH + hh) * NSPLIT + s0) * DM + col) * 4;
    const f32x4 pr0 = __builtin_bit_cast(f32x4, __builtin_amdgcn_raw_buffer_load_b128(wsr, po, 0, SC1));
    const f32x4 pr1 = __builtin_bit_cast(f32x4, __builtin_amdgcn_raw_buffer_load_b128(wsr, po + DM * 4, 0, SC1));
    const u32 mo = (u32)(WS_ML + (size_t)(rb * NH + hh) * NSPLIT * 2) * 4;
    const f32x4 ml0 = __builtin_bit_cast(f32x4, __builtin_amdgcn_raw_buffer_load_b128(wsr, mo, 0, SC1));
    const f32x4 ml1 = __builtin_bit_cast(f32x4, __builtin_amdgcn_raw_buffer_load_b128(wsr, mo + 16, 0, SC1));
    f32x4 fr[4];
#pragma unroll
    for (int j = 0; j < 4; ++j) {
      const int f = rg + 64 * j;
      const int fc = f < NFG ? f : NFG - 1;
      fr[j] = __builtin_bit_cast(f32x4, __builtin_amdgcn_raw_buffer_load_b128(wsr, (u32)(WS_F + (size_t)(fc * NB + rb) * DM + col) * 4, 0, SC1));
    }
    float gs = 0.f;
#pragma unroll
    for (int j = 0; j < 5; ++j) gs += rbf(bflo(xg[j]) * bflo(gg[j])) + rbf(bfhi(xg[j]) * bfhi(gg[j]));
    gs = rbf(xrow_sum(row_sum(gs)));
    const float g = rbf(1.f / (1.f + __expf(-gs)));
    const float M = fmaxf(fmaxf(ml0[0], ml0[2]), fmaxf(ml1[0], ml1[2]));
    const float e0 = __builtin_amdgcn_exp2f(ml0[0] - M), e1 = __builtin_amdgcn_exp2f(ml0[2] - M);
    const float e2 = __builtin_amdgcn_exp2f(ml1[0] - M), e3 = __builtin_amdgcn_exp2f(ml1[2] - M);
    const float inv = 1.f / (e0 * ml0[1] + e1 * ml0[3] + e2 * ml1[1] + e3 * ml1[3]);
    const float wa = (s0 ? e2 : e0) * inv, wb = (s0 ? e3 : e1) * inv;
    f32x4 a = wa * pr0 + wb * pr1;
    f32x4 f = fr[0] + fr[1] + fr[2];
    if (rg + 192 < NFG) f += fr[3];
#pragma unroll
    for (int k = 0; k < 4; ++k) {
      a[k] += dpp<XOR8>(a[k]); a[k] = xrow_sum(a[k]);
      f[k] += dpp<XOR8>(f[k]); f[k] = xrow_sum(f[k]);
    }
    if (lane < 8) {
      *reinterpret_cast<f32x4*>(&lds[wave * 64 + c4 * 4]) = a;
      *reinterpret_cast<f32x4*>(&lds[wave * 64 + 32 + c4 * 4]) = f;
    }
    __syncthreads();
    if (tid < 32) {
      float av = 0.f, fv = 0.f;
#pragma unroll
      for (int w = 0; w < NWAVE; ++w) { av += lds[w * 64 + tid]; fv += lds[w * 64 + 32 + tid]; }
      av = rbf(av); fv = rbf(fv);
      const float t1 = rbf(g * av), t2 = rbf(rbf(1.f - g) * fv);
      outp[rb * DM + cb * 32 + tid] = __builtin_bit_cast(u16, __float2bfloat16(t1 + t2));
    }
  }
}

}

extern "C" void kernel_launch(void* const* d_in, const int* in_sizes, int n_in, void* d_out, int out_size, void* d_ws,
                              size_t ws_size, hipStream_t stream) {
  const u16* x = (const u16*)d_in[0];
  const u16* kc = (const u16*)d_in[1];
  const u16* vc = (const u16*)d_in[2];
  const u16* wq = (const u16*)d_in[3];
  const u16* wo = (const u16*)d_in[4];
  const u16* w1 = (const u16*)d_in[6];
  const u16* w2 = (const u16*)d_in[7];
  const u16* gw = (const u16*)d_in[8];
  float* ws = (float*)d_ws;
  k_main<<<dim3(NB * NH * NSPLIT), dim3(512), 0, stream>>>(x, kc, vc, wq, wo, w1, w2, ws, gw, (u16*)d_out);
}
```

```cpp
#define KV_AUX 18
#include <hip/hip_runtime.h>
#include <hip/hip_bf16.h>
#include <stdint.h>

typedef unsigned int u32;
typedef unsigned short u16;
typedef __attribute__((ext_vector_type(4))) unsigned u32x4;
typedef __attribute__((ext_vector_type(2))) unsigned u32x2;
typedef __attribute__((ext_vector_type(4))) float f32x4;
typedef __attribute__((ext_vector_type(2))) float f32x2;
typedef __attribute__((ext_vector_type(2))) __bf16 bf16x2;

namespace {

constexpr int NB = 2, DM = 640, NH = 32, SK = 4096, DH = 128, FF = 1728;
constexpr int NSPLIT = 4, TOKWG = SK / NSPLIT, NWAVE = 8, TOKW = TOKWG / NWAVE;
constexpr int NFG = FF / 8;
constexpr float LOG2E = 1.4426950408889634f;
constexpr float SCALE = 0.088388347648318440f;

constexpr size_t WS_EPOCH = 0, WS_FLAG = 64;
constexpr size_t WS_P = WS_FLAG + 512;
constexpr size_t WS_ML = WS_P + (size_t)NB * NH * NSPLIT * DM;
constexpr size_t WS_F = WS_ML + (size_t)NB * NH * NSPLIT * 2;
constexpr size_t WS_END = WS_F + (size_t)NFG * NB * DM;
typedef __attribute__((address_space(1))) unsigned long long gu64;
#define RLX_AGENT __ATOMIC_RELAXED, __HIP_MEMORY_SCOPE_AGENT
constexpr int SC1 = 16;

__device__ __forceinline__ float dot2(u32 a, u32 b, float acc) {
  return __builtin_amdgcn_fdot2_f32_bf16(__builtin_bit_cast(bf16x2, a), __builtin_bit_cast(bf16x2, b), acc, false);
}
__device__ __forceinline__ float bflo(u32 v) { return __builtin_bit_cast(float, v << 16); }
__device__ __forceinline__ float bfhi(u32 v) { return __builtin_bit_cast(float, v & 0xffff0000u); }
__device__ __forceinline__ float rbf(float x) { return __bfloat162float(__float2bfloat16(x)); }
__device__ __forceinline__ u32 pk_bf(float lo, float hi) {
  const u32 a = __builtin_bit_cast(u16, __float2bfloat16(lo));
  const u32 b = __builtin_bit_cast(u16, __float2bfloat16(hi));
  return a | (b << 16);
}
template <int CTRL> __device__ __forceinline__ float dpp(float x) {
  return __builtin_bit_cast(float, __builtin_amdgcn_mov_dpp(__builtin_bit_cast(int, x), CTRL, 0xf, 0xf, true));
}
constexpr int XOR1 = 0xB1, XOR2 = 0x4E, XOR7 = 0x141, XOR8 = 0x128, BCAST = 0x150;

__device__ __forceinline__ float xrow_sum(float x) {
  auto s = __builtin_amdgcn_permlane16_swap(__float_as_uint(x), __float_as_uint(x), false, false);
  x = __uint_as_float(s[0]) + __uint_as_float(s[1]);
  auto t = __builtin_amdgcn_permlane32_swap(__float_as_uint(x), __float_as_uint(x), false, false);
  return __uint_as_float(t[0]) + __uint_as_float(t[1]);
}
__device__ __forceinline__ float xrow_max(float x) {
  auto s = __builtin_amdgcn_permlane16_swap(__float_as_uint(x), __float_as_uint(x), false, false);
  x = fmaxf(__uint_as_float(s[0]), __uint_as_float(s[1]));
  auto t = __builtin_amdgcn_permlane32_swap(__float_as_uint(x), __float_as_uint(x), false, false);
  return fmaxf(__uint_as_float(t[0]), __uint_as_float(t[1]));
}
__device__ __forceinline__ float row_sum(float x) {
  x += dpp<XOR1>(x); x += dpp<XOR2>(x); x += dpp<XOR7>(x); x += dpp<XOR8>(x); return x;
}
__device__ __forceinline__ float row_max(float x) {
  x = fmaxf(x, dpp<XOR1>(x)); x = fmaxf(x, dpp<XOR2>(x)); x = fmaxf(x, dpp<XOR7>(x)); x = fmaxf(x, dpp<XOR8>(x)); return x;
}
__device__ __forceinline__ float tr_reduce16(float (&x)[16], int c) {
#define TR_STEP(HALF, CTRL, BIT)                                                     \
  _Pragma("unroll") for (int h = 0; h < (HALF); ++h) {                               \
    const float lo_ = x[h], hi_ = x[h + (HALF)];                                     \
    const bool up_ = (c & (BIT)) != 0;                                               \
    const float send_ = up_ ? lo_ : hi_, keep_ = up_ ? hi_ : lo_;                    \
    x[h] = keep_ + dpp<CTRL>(send_);                                                 \
  }
  TR_STEP(8, XOR8, 8) TR_STEP(4, XOR7, 4) TR_STEP(2, XOR2, 2) TR_STEP(1, XOR1, 1)
#undef TR_STEP
  return x[0];
}

#define SCHED_FENCE() __builtin_amdgcn_sched_barrier(0)
#define STAMP(i) do {} while (0)

#define KV_NT 1
__device__ __forceinline__ u32x4 ld_nt(const u16* ub, u32 off) {
  return __builtin_nontemporal_load(reinterpret_cast<const u32x4*>(reinterpret_cast<const char*>(ub) + off));
}
#define W_NT 0
#define KV_PAT 1
__device__ __forceinline__ u32x4 ld16(const u16* ub, u32 off) {
  return *reinterpret_cast<const u32x4*>(reinterpret_cast<const char*>(ub) + off);
}
__device__ __forceinline__ u32x2 ld8(const u16* ub, u32 off) {
  return *reinterpret_cast<const u32x2*>(reinterpret_cast<const char*>(ub) + off);
}
__device__ __forceinline__ u32 ld2(const u16* ub, u32 off) {
  return *reinterpret_cast<const u16*>(reinterpret_cast<const char*>(ub) + off);
}

constexpr int L_QP = 0;
constexpr int L_F1 = L_QP + NWAVE * DH;
constexpr int L_OP = L_F1 + NWAVE * 16;
constexpr int L_ML = L_OP + NWAVE * DH;
constexpr int L_RED = L_ML + NWAVE * 2;
constexpr int L_END = L_RED + NWAVE * DM;

__global__ void __launch_bounds__(512, 2)
k_main(const u16* __restrict__ x, const u16* __restrict__ kc, const u16* __restrict__ vc, const u16* __restrict__ wq,
       const u16* __restrict__ wo, const u16* __restrict__ w1, const u16* __restrict__ w2, float* __restrict__ ws,
       const u16* __restrict__ gw, u16* __restrict__ outp) {
  __shared__ __attribute__((aligned(16))) float lds[L_END];
  const int bid = blockIdx.x, xcd = bid & 7, idx = bid >> 3;
  const int h = xcd * 4 + (idx >> 3), b = (idx >> 2) & 1, s = idx & 3;
  const int tid = threadIdx.x, lane = tid & 63, wave = __builtin_amdgcn_readfirstlane(tid >> 6);
  const int r = lane >> 4, c = lane & 15;
  const bool ffn_on = idx < 27;
  const int fg = xcd * 27 + (ffn_on ? idx : 26);

  STAMP(0);
  const __amdgpu_buffer_rsrc_t wsr = __builtin_amdgcn_make_buffer_rsrc(ws, 0, (int)(WS_END * 4), 0x00020000);
  const unsigned long long epoch = __hip_atomic_load((gu64*)(ws + WS_EPOCH), RLX_AGENT);
  SCHED_FENCE();
  u32x2 xq[5];
#pragma unroll
  for (int j = 0; j < 5; ++j) xq[j] = ld8(x + b * DM + wave * 80 + 4 * j, r * 40);
  SCHED_FENCE();
  u32x4 Wq[20], KV[64], Wo[20];
  constexpr int KVSTR = 16 * DH;
  const size_t kvbase = ((size_t)(b * NH + h) * SK + s * TOKWG + (wave >> 2) * (4 * TOKW) + (wave & 3) * 4) * DH;
  const u16* kp = kc + kvbase;
  const u16* vp = vc + kvbase;
  const u32 loff = lane * 16;
  const __amdgpu_buffer_rsrc_t krs = __builtin_amdgcn_make_buffer_rsrc((void*)kp, 0, 1 << 20, 0x00020000);
  const __amdgpu_buffer_rsrc_t vrs = __builtin_amdgcn_make_buffer_rsrc((void*)vp, 0, 1 << 20, 0x00020000);
#define LDKV(I) __builtin_amdgcn_raw_buffer_load_b128((I) < 32 ? krs : vrs, loff, ((I) & 31) * KVSTR * 2, KV_AUX)
  {
    const u32 off = (u32)(r * 20) * (NH * DH * 2) + 16 * c;
#pragma unroll
    for (int j = 0; j < 20; ++j) {
      Wq[j] = ld16(wq + (size_t)(wave * 80 + j) * (NH * DH) + h * DH, off);
      SCHED_FENCE();
      KV[j] = LDKV(j);
      SCHED_FENCE();
    }
  }
#pragma unroll
  for (int i = 20; i < 24; ++i) { KV[i] = LDKV(i); SCHED_FENCE(); }
  const u16* wop = wo + (size_t)(h * DH + wave * 16) * DM;
  const u32 wooff = (u32)r * (4 * DM * 2) + 16 * c;
#define PRE 5
#define ISSUE_T(T)                                                                                  \
  do {                                                                                              \
    if ((T) >= 0 && (T) < 20) {                                                                     \
      SCHED_FENCE();                                                                                \
      Wo[T] = ld16(wop + (2 * (((T) >> 1) / 5) + ((T) & 1)) * DM + (((T) >> 1) % 5) * 128, wooff);  \
      SCHED_FENCE();                                                                                \
      _Pragma("unroll") for (int t_ = 0; t_ < 2; ++t_) {                                            \
        const int kv_ = 24 + 2 * (T) + t_;                                                          \
        KV[kv_] = LDKV(kv_);                                                                        \
        SCHED_FENCE();                                                                              \
      }                                                                                             \
    }                                                                                               \
  } while (0)
#define PERM_Q(Q)                                                                                   \
  do {                                                                                              \
    _Pragma("unroll") for (int k_ = 0; k_ < 4; ++k_) {                                              \
      const u32 a_ = Wo[2 * (Q)][k_], b_ = Wo[2 * (Q) + 1][k_];                                     \
      Wo[2 * (Q)][k_] = __builtin_amdgcn_perm(b_, a_, 0x05040100u);                                 \
      Wo[2 * (Q) + 1][k_] = __builtin_amdgcn_perm(b_, a_, 0x07060302u);                             \
    }                                                                                               \
  } while (0)
#define AFTER_KV(I)                                                                                 \
  do {                                                                                              \
    if ((I) % 3 == 2) ISSUE_T(PRE + (I) / 3);                                                       \
    if ((I) >= 26 && ((I) - 26) % 4 == 0) PERM_Q(((I) - 26) / 4);                                   \
  } while (0)

#define ABAR 0
  STAMP(1);
  {
    float qa[8];
#pragma unroll
    for (int k = 0; k < 8; ++k) qa[k] = 0.f;
#pragma unroll
    for (int j = 0; j < 20; ++j) {
      const u32 xw = xq[j >> 2][(j >> 1) & 1];
      const float xv = (j & 1) ? bfhi(xw) : bflo(xw);
#pragma unroll
      for (int k = 0; k < 4; ++k) {
        qa[2 * k] = fmaf(xv, bflo(Wq[j][k]), qa[2 * k]);
        qa[2 * k + 1] = fmaf(xv, bfhi(Wq[j][k]), qa[2 * k + 1]);
      }
    }
#pragma unroll
    for (int k = 0; k < 8; ++k) qa[k] = xrow_sum(qa[k]);
    if (r == 0) {
      *reinterpret_cast<f32x4*>(&lds[L_QP + wave * DH + 8 * c]) = f32x4{qa[0], qa[1], qa[2], qa[3]};
      *reinterpret_cast<f32x4*>(&lds[L_QP + wave * DH + 8 * c + 4]) = f32x4{qa[4], qa[5], qa[6], qa[7]};
    }
  }
  SCHED_FENCE();
  STAMP(2);
  const u32x4 w1a = ld16(w1 + (size_t)(wave * 80) * FF + fg * 8, (u32)lane * (FF * 2));
  const u32x4 w1b = ld16(w1 + (size_t)(wave * 80 + 64) * FF + fg * 8, (u32)c * (FF * 2));
  const u32 xa0 = ld2(x + wave * 80, 2 * lane), xa1 = ld2(x + DM + wave * 80, 2 * lane);
  const u32 xb0 = ld2(x + wave * 80 + 64, 2 * c), xb1 = ld2(x + DM + wave * 80 + 64, 2 * c);
  SCHED_FENCE();
#pragma unroll
  for (int T = 0; T < PRE; ++T) ISSUE_T(T);
  __syncthreads();
  STAMP(3);
  u32 qpk[4];
  {
    f32x4 q0 = {0.f, 0.f, 0.f, 0.f}, q1 = {0.f, 0.f, 0.f, 0.f};
#pragma unroll
    for (int w = 0; w < NWAVE; ++w) {
      q0 += *reinterpret_cast<const f32x4*>(&lds[L_QP + w * DH + 8 * c]);
      q1 += *reinterpret_cast<const f32x4*>(&lds[L_QP + w * DH + 8 * c + 4]);
    }
    qpk[0] = pk_bf(q0[0], q0[1]); qpk[1] = pk_bf(q0[2], q0[3]);
    qpk[2] = pk_bf(q1[0], q1[1]); qpk[3] = pk_bf(q1[2], q1[3]);
  }

  float sc[2];
  u32x2 w2r[8];
#pragma unroll
  for (int g = 0; g < 2; ++g) {
    float xx[16];
#pragma unroll
    for (int i = 0; i < 16; ++i) {
      const u32x4 kk = KV[g * 16 + i];
      float d = dot2(kk[0], qpk[0], 0.f);
      d = dot2(kk[1], qpk[1], d);
      d = dot2(kk[2], qpk[2], d);
      xx[i] = dot2(kk[3], qpk[3], d);
      AFTER_KV(g * 16 + i);
    }
    STAMP(4 + g);
    const float t = tr_reduce16(xx, c);
    sc[g] = rbf(rbf(t) * SCALE);
    if (g == 0) {
      SCHED_FENCE();
      float fa[16];
      const float a0 = bflo(xa0), a1 = bflo(xa1);
      const float b0 = lane < 16 ? bflo(xb0) : 0.f, b1 = lane < 16 ? bflo(xb1) : 0.f;
#pragma unroll
      for (int k = 0; k < 4; ++k) {
        fa[2 * k] = fmaf(a0, bflo(w1a[k]), b0 * bflo(w1b[k]));
        fa[2 * k + 1] = fmaf(a0, bfhi(w1a[k]), b0 * bfhi(w1b[k]));
        fa[8 + 2 * k] = fmaf(a1, bflo(w1a[k]), b1 * bflo(w1b[k]));
        fa[8 + 2 * k + 1] = fmaf(a1, bfhi(w1a[k]), b1 * bfhi(w1b[k]));
      }
      float ft = tr_reduce16(fa, c);
      ft = xrow_sum(ft);
      if (r == 0) lds[L_F1 + wave * 16 + c] = ft;
      SCHED_FENCE();
      const u32 col = (tid < 160 ? tid : 0) * 8;
#pragma unroll
      for (int j = 0; j < 8; ++j) w2r[j] = ld8(w2 + (size_t)(fg * 8 + j) * DM, col);
      SCHED_FENCE();
    }
  }
  {
    const float mw = xrow_max(row_max(fmaxf(sc[0], sc[1])));
    if (lane == 0) lds[L_ML + wave] = mw;
  }
  __syncthreads();
  float m2;
  {
    const f32x4 t0 = *reinterpret_cast<const f32x4*>(&lds[L_ML]), t1 = *reinterpret_cast<const f32x4*>(&lds[L_ML + 4]);
    m2 = fmaxf(fmaxf(fmaxf(t0[0], t0[1]), fmaxf(t0[2], t0[3])), fmaxf(fmaxf(t1[0], t1[1]), fmaxf(t1[2], t1[3]))) * LOG2E;
  }
  float p[2];
  p[0] = __builtin_amdgcn_exp2f(fmaf(sc[0], LOG2E, -m2));
  p[1] = __builtin_amdgcn_exp2f(fmaf(sc[1], LOG2E, -m2));
  const float lsum = xrow_sum(row_sum(p[0] + p[1]));
  if (lane == 0) lds[L_ML + 8 + wave] = lsum;

  {
    float hval;
    float t = 0.f;
#pragma unroll
    for (int w = 0; w < NWAVE; ++w) t += lds[L_F1 + w * 16 + c];
    t = rbf(t);
    hval = rbf(t / (1.f + __expf(-t)));
    float f0[4] = {0.f, 0.f, 0.f, 0.f}, f1[4] = {0.f, 0.f, 0.f, 0.f};
#define FFN2_STEP(J)                                                                  \
    {                                                                                 \
      const float h0 = dpp<BCAST + (J)>(hval), h1 = dpp<BCAST + 8 + (J)>(hval);       \
      const float w0 = bflo(w2r[J][0]), w1_ = bfhi(w2r[J][0]), w2_ = bflo(w2r[J][1]), w3 = bfhi(w2r[J][1]); \
      f0[0] = fmaf(h0, w0, f0[0]); f0[1] = fmaf(h0, w1_, f0[1]); f0[2] = fmaf(h0, w2_, f0[2]); f0[3] = fmaf(h0, w3, f0[3]); \
      f1[0] = fmaf(h1, w0, f1[0]); f1[1] = fmaf(h1, w1_, f1[1]); f1[2] = fmaf(h1, w2_, f1[2]); f1[3] = fmaf(h1, w3, f1[3]); \
    }
    FFN2_STEP(0) FFN2_STEP(1) FFN2_STEP(2) FFN2_STEP(3) FFN2_STEP(4) FFN2_STEP(5) FFN2_STEP(6) FFN2_STEP(7)
#undef FFN2_STEP
    asm volatile("" : "+v"(f0[0]), "+v"(f0[1]), "+v"(f0[2]), "+v"(f0[3]), "+v"(f1[0]), "+v"(f1[1]), "+v"(f1[2]), "+v"(f1[3]));
    if (ffn_on && tid < 160) {
      const u32 fo = (u32)(WS_F + (size_t)fg * NB * DM + tid * 4) * 4;
      __builtin_amdgcn_raw_buffer_store_b128(__builtin_bit_cast(u32x4, f32x4{f0[0], f0[1], f0[2], f0[3]}), wsr, fo, 0, SC1);
      __builtin_amdgcn_raw_buffer_store_b128(__builtin_bit_cast(u32x4, f32x4{f1[0], f1[1], f1[2], f1[3]}), wsr, fo + DM * 4, 0, SC1);
    }
  }
  SCHED_FENCE();

  float acc[8];
#pragma unroll
  for (int k = 0; k < 8; ++k) acc[k] = 0.f;
#define PV_STEP(I)                                                                    \
  {                                                                                   \
    const float pb = dpp<BCAST + ((I) & 15)>(p[(I) >> 4]);                            \
    const u32x4 vv = KV[32 + (I)];                                                    \
    _Pragma("unroll") for (int k = 0; k < 4; ++k) {                                   \
      acc[2 * k] = fmaf(pb, bflo(vv[k]), acc[2 * k]);                                 \
      acc[2 * k + 1] = fmaf(pb, bfhi(vv[k]), acc[2 * k + 1]);                         \
    }                                                                                 \
    AFTER_KV(32 + (I));                                                               \
  }
  PV_STEP(0) PV_STEP(1) PV_STEP(2) PV_STEP(3) PV_STEP(4) PV_STEP(5) PV_STEP(6) PV_STEP(7)
  PV_STEP(8) PV_STEP(9) PV_STEP(10) PV_STEP(11) PV_STEP(12) PV_STEP(13) PV_STEP(14) PV_STEP(15)
  PV_STEP(16) PV_STEP(17) PV_STEP(18) PV_STEP(19) PV_STEP(20) PV_STEP(21) PV_STEP(22) PV_STEP(23)
  PV_STEP(24) PV_STEP(25) PV_STEP(26) PV_STEP(27) PV_STEP(28) PV_STEP(29) PV_STEP(30) PV_STEP(31)
#undef PV_STEP
  STAMP(6);
#pragma unroll
  for (int k = 0; k < 8; ++k) acc[k] = xrow_sum(acc[k]);
  if (r == 0) {
    *reinterpret_cast<f32x4*>(&lds[L_OP + wave * DH + 8 * c]) = f32x4{acc[0], acc[1], acc[2], acc[3]};
    *reinterpret_cast<f32x4*>(&lds[L_OP + wave * DH + 8 * c + 4]) = f32x4{acc[4], acc[5], acc[6], acc[7]};
  }
  __syncthreads();

  STAMP(7);
  {
    f32x4 o4 = {0.f, 0.f, 0.f, 0.f};
#pragma unroll
    for (int w = 0; w < 8; ++w) o4 += *reinterpret_cast<const f32x4*>(&lds[L_OP + w * DH + (wave * 4 + r) * 4]);
    if (tid == 0) {
      const f32x4 t0 = *reinterpret_cast<const f32x4*>(&lds[L_ML + 8]), t1 = *reinterpret_cast<const f32x4*>(&lds[L_ML + 12]);
      const float L = ((t0[0] + t0[1]) + (t0[2] + t0[3])) + ((t1[0] + t1[1]) + (t1[2] + t1[3]));
      __builtin_amdgcn_raw_buffer_store_b64(__builtin_bit_cast(u32x2, f32x2{m2, L}), wsr, (u32)(WS_ML + (size_t)((b * NH + h) * NSPLIT + s) * 2) * 4, 0, SC1);
    }
    const u32 o01 = pk_bf(o4[0], o4[1]), o23 = pk_bf(o4[2], o4[3]);
    float y[40];
#pragma unroll
    for (int seg = 0; seg < 5; ++seg)
#pragma unroll
      for (int k = 0; k < 4; ++k) {
        y[(2 * k) * 5 + seg] = dot2(Wo[2 * (5 + seg)][k], o23, dot2(Wo[2 * seg][k], o01, 0.f));
        y[(2 * k + 1) * 5 + seg] = dot2(Wo[2 * (5 + seg) + 1][k], o23, dot2(Wo[2 * seg + 1][k], o01, 0.f));
      }
    float z[20];
#pragma unroll
    for (int v = 0; v < 20; ++v) {
      auto t = __builtin_amdgcn_permlane32_swap(__float_as_uint(y[v]), __float_as_uint(y[v + 20]), false, false);
      z[v] = __uint_as_float(t[0]) + __uint_as_float(t[1]);
    }
    float u[10];
#pragma unroll
    for (int v = 0; v < 10; ++v) {
      auto t = __builtin_amdgcn_permlane16_swap(__float_as_uint(z[v]), __float_as_uint(z[v + 10]), false, false);
      u[v] = __uint_as_float(t[0]) + __uint_as_float(t[1]);
    }
#pragma unroll
    for (int seg = 0; seg < 5; ++seg)
      *reinterpret_cast<f32x2*>(&lds[L_RED + wave * DM + seg * 128 + 8 * c + 2 * r]) = f32x2{u[seg], u[5 + seg]};
  }
  __syncthreads();
  if (tid < 160) {
    f32x4 t = {0.f, 0.f, 0.f, 0.f};
#pragma unroll
    for (int w = 0; w < NWAVE; ++w) t += *reinterpret_cast<const f32x4*>(&lds[L_RED + w * DM + tid * 4]);
    __builtin_amdgcn_raw_buffer_store_b128(__builtin_bit_cast(u32x4, t), wsr, (u32)(WS_P + (size_t)((b * NH + h) * NSPLIT + s) * DM + tid * 4) * 4, 0, SC1);
  }
  asm volatile("s_waitcnt vmcnt(0)" ::: "memory");
  __syncthreads();
  const unsigned long long tag = epoch + 1ull;
  if (tid == 0) __hip_atomic_store((gu64*)(ws + WS_FLAG) + bid, tag, RLX_AGENT);
  STAMP(8);

  if (bid < NB * 20) {
    const int rb = bid / 20, cb = bid % 20;
    u32 xg[5], gg[5];
#pragma unroll
    for (int j = 0; j < 5; ++j) {
      xg[j] = *reinterpret_cast<const u32*>(x + rb * DM + 2 * (lane + 64 * j));
      gg[j] = *reinterpret_cast<const u32*>(gw + 2 * (lane + 64 * j));
    }
    if (wave == 0) {
      gu64* fl = (gu64*)(ws + WS_FLAG) + 4 * lane;
      for (unsigned spins = 0; spins < (1u << 22); ++spins) {
        const unsigned long long f0 = __hip_atomic_load(fl, RLX_AGENT), f1 = __hip_atomic_load(fl + 1, RLX_AGENT);
        const unsigned long long f2 = __hip_atomic_load(fl + 2, RLX_AGENT), f3 = __hip_atomic_load(fl + 3, RLX_AGENT);
        if (__all(f0 == tag && f1 == tag && f2 == tag && f3 == tag)) break;
        __builtin_amdgcn_s_sleep(1);
      }
      if (bid == 0 && lane == 0) __hip_atomic_store((gu64*)(ws + WS_EPOCH), tag, RLX_AGENT);
    }
    asm volatile("" ::: "memory");
    __syncthreads();
    const int c4 = tid & 7, rg = tid >> 3;
    const int col = cb * 32 + c4 * 4;
    const int hh = rg >> 1, s0 = (rg & 1) * 2;
    const u32 po = (u32)(WS_P + (size_t)((rb * NH + hh) * NSPLIT + s0) * DM + col) * 4;
    const f32x4 pr0 = __builtin_bit_cast(f32x4, __builtin_amdgcn_raw_buffer_load_b128(wsr, po, 0, SC1));
    const f32x4 pr1 = __builtin_bit_cast(f32x4, __builtin_amdgcn_raw_buffer_load_b128(wsr, po + DM * 4, 0, SC1));
    const u32 mo = (u32)(WS_ML + (size_t)(rb * NH + hh) * NSPLIT * 2) * 4;
    const f32x4 ml0 = __builtin_bit_cast(f32x4, __builtin_amdgcn_raw_buffer_load_b128(wsr, mo, 0, SC1));
    const f32x4 ml1 = __builtin_bit_cast(f32x4, __builtin_amdgcn_raw_buffer_load_b128(wsr, mo + 16, 0, SC1));
    f32x4 fr[4];
#pragma unroll
    for (int j = 0; j < 4; ++j) {
      const int f = rg + 64 * j;
      const int fc = f < NFG ? f : NFG - 1;
      fr[j] = __builtin_bit_cast(f32x4, __builtin_amdgcn_raw_buffer_load_b128(wsr, (u32)(WS_F + (size_t)(fc * NB + rb) * DM + col) * 4, 0, SC1));
    }
    float gs = 0.f;
#pragma unroll
    for (int j = 0; j < 5; ++j) gs += rbf(bflo(xg[j]) * bflo(gg[j])) + rbf(bfhi(xg[j]) * bfhi(gg[j]));
    gs = rbf(xrow_sum(row_sum(gs)));
    const float g = rbf(1.f / (1.f + __expf(-gs)));
    const float M = fmaxf(fmaxf(ml0[0], ml0[2]), fmaxf(ml1[0], ml1[2]));
    const float e0 = __builtin_amdgcn_exp2f(ml0[0] - M), e1 = __builtin_amdgcn_exp2f(ml0[2] - M);
    const float e2 = __builtin_amdgcn_exp2f(ml1[0] - M), e3 = __builtin_amdgcn_exp2f(ml1[2] - M);
    const float inv = 1.f / (e0 * ml0[1] + e1 * ml0[3] + e2 * ml1[1] + e3 * ml1[3]);
    const float wa = (s0 ? e2 : e0) * inv, wb = (s0 ? e3 : e1) * inv;
    f32x4 a = wa * pr0 + wb * pr1;
    f32x4 f = fr[0] + fr[1] + fr[2];
    if (rg + 192 < NFG) f += fr[3];
#pragma unroll
    for (int k = 0; k < 4; ++k) {
      a[k] += dpp<XOR8>(a[k]); a[k] = xrow_sum(a[k]);
      f[k] += dpp<XOR8>(f[k]); f[k] = xrow_sum(f[k]);
    }
    if (lane < 8) {
      *reinterpret_cast<f32x4*>(&lds[wave * 64 + c4 * 4]) = a;
      *reinterpret_cast<f32x4*>(&lds[wave * 64 + 32 + c4 * 4]) = f;
    }
    __syncthreads();
    if (tid < 32) {
      float av = 0.f, fv = 0.f;
#pragma unroll
      for (int w = 0; w < NWAVE; ++w) { av += lds[w * 64 + tid]; fv += lds[w * 64 + 32 + tid]; }
      av = rbf(av); fv = rbf(fv);
      const float t1 = rbf(g * av), t2 = rbf(rbf(1.f - g) * fv);
      outp[rb * DM + cb * 32 + tid] = __builtin_bit_cast(u16, __float2bfloat16(t1 + t2));
    }
  }
}

}

extern "C" void kernel_launch(void* const* d_in, const int* in_sizes, int n_in, void* d_out, int out_size, void* d_ws,
                              size_t ws_size, hipStream_t stream) {
  const u16* x = (const u16*)d_in[0];
  const u16* kc = (const u16*)d_in[1];
  const u16* vc = (const u16*)d_in[2];
  const u16* wq = (const u16*)d_in[3];
  const u16* wo = (const u16*)d_in[4];
  const u16* w1 = (const u16*)d_in[6];
  const u16* w2 = (const u16*)d_in[7];
  const u16* gw = (const u16*)d_in[8];
  float* ws = (float*)d_ws;
  k_main<<<dim3(NB * NH * NSPLIT), dim3(512), 0, stream>>>(x, kc, vc, wq, wo, w1, w2, ws, gw, (u16*)d_out);
}
```

```cpp
#include <hip/hip_runtime.h>
#include <hip/hip_bf16.h>
#include <stdint.h>

typedef unsigned int u32;
typedef unsigned short u16;
typedef __attribute__((ext_vector_type(4))) unsigned u32x4;
typedef __attribute__((ext_vector_type(2))) unsigned u32x2;
typedef __attribute__((ext_vector_type(4))) float f32x4;
typedef __attribute__((ext_vector_type(2))) float f32x2;
typedef __attribute__((ext_vector_type(2))) __bf16 bf16x2;

namespace {

constexpr int NB = 2, DM = 640, NH = 32, SK = 4096, DH = 128, FF = 1728;
constexpr int NSPLIT = 4, TOKWG = SK / NSPLIT, NWAVE = 8, TOKW = TOKWG / NWAVE;
constexpr int NFG = FF / 8;
constexpr float LOG2E = 1.4426950408889634f;
constexpr float SCALE = 0.088388347648318440f;

constexpr size_t WS_EPOCH = 0, WS_FLAG = 64;
constexpr size_t WS_P = WS_FLAG + 512;
constexpr size_t WS_ML = WS_P + (size_t)NB * NH * NSPLIT * DM;
constexpr size_t WS_F = WS_ML + (size_t)NB * NH * NSPLIT * 2;
constexpr size_t WS_END = WS_F + (size_t)NFG * NB * DM;
typedef __attribute__((address_space(1))) unsigned long long gu64;
#define RLX_AGENT __ATOMIC_RELAXED, __HIP_MEMORY_SCOPE_AGENT
constexpr int SC1 = 16;

__device__ __forceinline__ float dot2(u32 a, u32 b, float acc) {
  return __builtin_amdgcn_fdot2_f32_bf16(__builtin_bit_cast(bf16x2, a), __builtin_bit_cast(bf16x2, b), acc, false);
}
__device__ __forceinline__ float bflo(u32 v) { return __builtin_bit_cast(float, v << 16); }
__device__ __forceinline__ float bfhi(u32 v) { return __builtin_bit_cast(float, v & 0xffff0000u); }
__device__ __forceinline__ float rbf(float x) { return __bfloat162float(__float2bfloat16(x)); }
__device__ __forceinline__ u32 pk_bf(float lo, float hi) {
  const u32 a = __builtin_bit_cast(u16, __float2bfloat16(lo));
  const u32 b = __builtin_bit_cast(u16, __float2bfloat16(hi));
  return a | (b << 16);
}
template <int CTRL> __device__ __forceinline__ float dpp(float x) {
  return __builtin_bit_cast(float, __builtin_amdgcn_mov_dpp(__builtin_bit_cast(int, x), CTRL, 0xf, 0xf, true));
}
constexpr int XOR1 = 0xB1, XOR2 = 0x4E, XOR7 = 0x141, XOR8 = 0x128, BCAST = 0x150;

__device__ __forceinline__ float xrow_sum(float x) {
  auto s = __builtin_amdgcn_permlane16_swap(__float_as_uint(x), __float_as_uint(x), false, false);
  x = __uint_as_float(s[0]) + __uint_as_float(s[1]);
  auto t = __builtin_amdgcn_permlane32_swap(__float_as_uint(x), __float_as_uint(x), false, false);
  return __uint_as_float(t[0]) + __uint_as_float(t[1]);
}
__device__ __forceinline__ float xrow_max(float x) {
  auto s = __builtin_amdgcn_permlane16_swap(__float_as_uint(x), __float_as_uint(x), false, false);
  x = fmaxf(__uint_as_float(s[0]), __uint_as_float(s[1]));
  auto t = __builtin_amdgcn_permlane32_swap(__float_as_uint(x), __float_as_uint(x), false, false);
  return fmaxf(__uint_as_float(t[0]), __uint_as_float(t[1]));
}
__device__ __forceinline__ float row_sum(float x) {
  x += dpp<XOR1>(x); x += dpp<XOR2>(x); x += dpp<XOR7>(x); x += dpp<XOR8>(x); return x;
}
__device__ __forceinline__ float row_max(float x) {
  x = fmaxf(x, dpp<XOR1>(x)); x = fmaxf(x, dpp<XOR2>(x)); x = fmaxf(x, dpp<XOR7>(x)); x = fmaxf(x, dpp<XOR8>(x)); return x;
}
__device__ __forceinline__ float tr_reduce16(float (&x)[16], int c) {
#define TR_STEP(HALF, CTRL, BIT)                                                     \
  _Pragma("unroll") for (int h = 0; h < (HALF); ++h) {                               \
    const float lo_ = x[h], hi_ = x[h + (HALF)];                                     \
    const bool up_ = (c & (BIT)) != 0;                                               \
    const float send_ = up_ ? lo_ : hi_, keep_ = up_ ? hi_ : lo_;                    \
    x[h] = keep_ + dpp<CTRL>(send_);                                                 \
  }
  TR_STEP(8, XOR8, 8) TR_STEP(4, XOR7, 4) TR_STEP(2, XOR2, 2) TR_STEP(1, XOR1, 1)
#undef TR_STEP
  return x[0];
}

#define SCHED_FENCE() __builtin_amdgcn_sched_barrier(0)
#define STAMP(i) do {} while (0)

#define KV_NT 1
__device__ __forceinline__ u32x4 ld_nt(const u16* ub, u32 off) {
  return __builtin_nontemporal_load(reinterpret_cast<const u32x4*>(reinterpret_cast<const char*>(ub) + off));
}
#define W_NT 0
#define KV_PAT 1
__device__ __forceinline__ u32x4 ld16(const u16* ub, u32 off) {
  return *reinterpret_cast<const u32x4*>(reinterpret_cast<const char*>(ub) + off);
}
__device__ __forceinline__ u32x2 ld8(const u16* ub, u32 off) {
  return *reinterpret_cast<const u32x2*>(reinterpret_cast<const char*>(ub) + off);
}
__device__ __forceinline__ u32 ld2(const u16* ub, u32 off) {
  return *reinterpret_cast<const u16*>(reinterpret_cast<const char*>(ub) + off);
}

constexpr int L_QP = 0;
constexpr int L_F1 = L_QP + NWAVE * DH;
constexpr int L_OP = L_F1 + NWAVE * 16;
constexpr int L_ML = L_OP + NWAVE * DH;
constexpr int L_RED = L_ML + NWAVE * 2;
constexpr int L_END = L_RED + NWAVE * DM;

__global__ void __launch_bounds__(512, 2)
k_main(const u16* __restrict__ x, const u16* __restrict__ kc, const u16* __restrict__ vc, const u16* __restrict__ wq,
       const u16* __restrict__ wo, const u16* __restrict__ w1, const u16* __restrict__ w2, float* __restrict__ ws,
       const u16* __restrict__ gw, u16* __restrict__ outp) {
  __shared__ __attribute__((aligned(16))) float lds[L_END];
  const int bid = blockIdx.x, xcd = bid & 7, idx = bid >> 3;
  const int h = xcd * 4 + (idx >> 3), b = (idx >> 2) & 1, s = idx & 3;
  const int tid = threadIdx.x, lane = tid & 63, wave = __builtin_amdgcn_readfirstlane(tid >> 6);
  const int r = lane >> 4, c = lane & 15;
  const bool ffn_on = idx < 27;
  const int fg = xcd * 27 + (ffn_on ? idx : 26);

  STAMP(0);
  const __amdgpu_buffer_rsrc_t wsr = __builtin_amdgcn_make_buffer_rsrc(ws, 0, (int)(WS_END * 4), 0x00020000);
  const unsigned long long epoch = __hip_atomic_load((gu64*)(ws + WS_EPOCH), RLX_AGENT);
  SCHED_FENCE();
  u32x2 xq[5];
#pragma unroll
  for (int j = 0; j < 5; ++j) xq[j] = ld8(x + b * DM + wave * 80 + 4 * j, r * 40);
  SCHED_FENCE();
  u32x4 Wq[20], KV[64], Wo[20];
  constexpr int KVSTR = 16 * DH;
  const size_t kvbase = ((size_t)(b * NH + h) * SK + s * TOKWG + (wave >> 2) * (4 * TOKW) + (wave & 3) * 4) * DH;
  const u16* kp = kc + kvbase;
  const u16* vp = vc + kvbase;
  const u32 loff = lane * 16;
#define LDKV(I) ld_nt(((I) < 32 ? kp + (I) * KVSTR : vp + ((I) - 32) * KVSTR), loff)
  {
    const u32 off = (u32)(r * 20) * (NH * DH * 2) + 16 * c;
#pragma unroll
    for (int j = 0; j < 20; ++j) {
      Wq[j] = ld16(wq + (size_t)(wave * 80 + j) * (NH * DH) + h * DH, off);
      SCHED_FENCE();
      KV[j] = LDKV(j);
      SCHED_FENCE();
    }
  }
#pragma unroll
  for (int i = 20; i < 24; ++i) { KV[i] = LDKV(i); SCHED_FENCE(); }
  const u16* wop = wo + (size_t)(h * DH + wave * 16) * DM;
  const u32 wooff = (u32)r * (4 * DM * 2) + 16 * c;
#define PRE 5
#define ISSUE_T(T)                                                                                  \
  do {                                                                                              \
    if ((T) >= 0 && (T) < 20) {                                                                     \
      SCHED_FENCE();                                                                                \
      Wo[T] = ld16(wop + (2 * (((T) >> 1) / 5) + ((T) & 1)) * DM + (((T) >> 1) % 5) * 128, wooff);  \
      SCHED_FENCE();                                                                                \
      _Pragma("unroll") for (int t_ = 0; t_ < 2; ++t_) {                                            \
        const int kv_ = 24 + 2 * (T) + t_;                                                          \
        KV[kv_] = LDKV(kv_);                                                                        \
        SCHED_FENCE();                                                                              \
      }                                                                                             \
    }                                                                                               \
  } while (0)
#define PERM_Q(Q)                                                                                   \
  do {                                                                                              \
    _Pragma("unroll") for (int k_ = 0; k_ < 4; ++k_) {                                              \
      const u32 a_ = Wo[2 * (Q)][k_], b_ = Wo[2 * (Q) + 1][k_];                                     \
      Wo[2 * (Q)][k_] = __builtin_amdgcn_perm(b_, a_, 0x05040100u);                                 \
      Wo[2 * (Q) + 1][k_] = __builtin_amdgcn_perm(b_, a_, 0x07060302u);                             \
    }                                                                                               \
  } while (0)
#define AFTER_KV(I)                                                                                 \
  do {                                                                                              \
    if ((I) % 3 == 2) ISSUE_T(PRE + (I) / 3);                                                       \
    if ((I) >= 26 && ((I) - 26) % 4 == 0) PERM_Q(((I) - 26) / 4);                                   \
  } while (0)

#define ABAR 0
  STAMP(1);
  {
    float qa[8];
#pragma unroll
    for (int k = 0; k < 8; ++k) qa[k] = 0.f;
#pragma unroll
    for (int j = 0; j < 20; ++j) {
      const u32 xw = xq[j >> 2][(j >> 1) & 1];
      const float xv = (j & 1) ? bfhi(xw) : bflo(xw);
#pragma unroll
      for (int k = 0; k < 4; ++k) {
        qa[2 * k] = fmaf(xv, bflo(Wq[j][k]), qa[2 * k]);
        qa[2 * k + 1] = fmaf(xv, bfhi(Wq[j][k]), qa[2 * k + 1]);
      }
    }
#pragma unroll
    for (int k = 0; k < 8; ++k) qa[k] = xrow_sum(qa[k]);
    if (r == 0) {
      *reinterpret_cast<f32x4*>(&lds[L_QP + wave * DH + 8 * c]) = f32x4{qa[0], qa[1], qa[2], qa[3]};
      *reinterpret_cast<f32x4*>(&lds[L_QP + wave * DH + 8 * c + 4]) = f32x4{qa[4], qa[5], qa[6], qa[7]};
    }
  }
  SCHED_FENCE();
  STAMP(2);
  const u32x4 w1a = ld16(w1 + (size_t)(wave * 80) * FF + fg * 8, (u32)lane * (FF * 2));
  const u32x4 w1b = ld16(w1 + (size_t)(wave * 80 + 64) * FF + fg * 8, (u32)c * (FF * 2));
  const u32 xa0 = ld2(x + wave * 80, 2 * lane), xa1 = ld2(x + DM + wave * 80, 2 * lane);
  const u32 xb0 = ld2(x + wave * 80 + 64, 2 * c), xb1 = ld2(x + DM + wave * 80 + 64, 2 * c);
  SCHED_FENCE();
#pragma unroll
  for (int T = 0; T < PRE; ++T) ISSUE_T(T);
  __syncthreads();
  STAMP(3);
  u32 qpk[4];
  {
    f32x4 q0 = {0.f, 0.f, 0.f, 0.f}, q1 = {0.f, 0.f, 0.f, 0.f};
#pragma unroll
    for (int w = 0; w < NWAVE; ++w) {
      q0 += *reinterpret_cast<const f32x4*>(&lds[L_QP + w * DH + 8 * c]);
      q1 += *reinterpret_cast<const f32x4*>(&lds[L_QP + w * DH + 8 * c + 4]);
    }
    qpk[0] = pk_bf(q0[0], q0[1]); qpk[1] = pk_bf(q0[2], q0[3]);
    qpk[2] = pk_bf(q1[0], q1[1]); qpk[3] = pk_bf(q1[2], q1[3]);
  }

  float sc[2];
  u32x2 w2r[8];
#pragma unroll
  for (int g = 0; g < 2; ++g) {
    float xx[16];
#pragma unroll
    for (int i = 0; i < 16; ++i) {
      const u32x4 kk = KV[g * 16 + i];
      float d = dot2(kk[0], qpk[0], 0.f);
      d = dot2(kk[1], qpk[1], d);
      d = dot2(kk[2], qpk[2], d);
      xx[i] = dot2(kk[3], qpk[3], d);
      AFTER_KV(g * 16 + i);
    }
    STAMP(4 + g);
    const float t = tr_reduce16(xx, c);
    sc[g] = rbf(rbf(t) * SCALE);
    if (g == 0) {
      SCHED_FENCE();
      float fa[16];
      const float a0 = bflo(xa0), a1 = bflo(xa1);
      const float b0 = lane < 16 ? bflo(xb0) : 0.f, b1 = lane < 16 ? bflo(xb1) : 0.f;
#pragma unroll
      for (int k = 0; k < 4; ++k) {
        fa[2 * k] = fmaf(a0, bflo(w1a[k]), b0 * bflo(w1b[k]));
        fa[2 * k + 1] = fmaf(a0, bfhi(w1a[k]), b0 * bfhi(w1b[k]));
        fa[8 + 2 * k] = fmaf(a1, bflo(w1a[k]), b1 * bflo(w1b[k]));
        fa[8 + 2 * k + 1] = fmaf(a1, bfhi(w1a[k]), b1 * bfhi(w1b[k]));
      }
      float ft = tr_reduce16(fa, c);
      ft = xrow_sum(ft);
      if (r == 0) lds[L_F1 + wave * 16 + c] = ft;
      SCHED_FENCE();
      const u32 col = (tid < 160 ? tid : 0) * 8;
#pragma unroll
      for (int j = 0; j < 8; ++j) w2r[j] = ld8(w2 + (size_t)(fg * 8 + j) * DM, col);
      SCHED_FENCE();
    }
  }
  {
    const float mw = xrow_max(row_max(fmaxf(sc[0], sc[1])));
    if (lane == 0) lds[L_ML + wave] = mw;
  }
  __syncthreads();
  float m2;
  {
    const f32x4 t0 = *reinterpret_cast<const f32x4*>(&lds[L_ML]), t1 = *reinterpret_cast<const f32x4*>(&lds[L_ML + 4]);
    m2 = fmaxf(fmaxf(fmaxf(t0[0], t0[1]), fmaxf(t0[2], t0[3])), fmaxf(fmaxf(t1[0], t1[1]), fmaxf(t1[2], t1[3]))) * LOG2E;
  }
  float p[2];
  p[0] = __builtin_amdgcn_exp2f(fmaf(sc[0], LOG2E, -m2));
  p[1] = __builtin_amdgcn_exp2f(fmaf(sc[1], LOG2E, -m2));
  const float lsum = xrow_sum(row_sum(p[0] + p[1]));
  if (lane == 0) lds[L_ML + 8 + wave] = lsum;

  {
    float hval;
    float t = 0.f;
#pragma unroll
    for (int w = 0; w < NWAVE; ++w) t += lds[L_F1 + w * 16 + c];
    t = rbf(t);
    hval = rbf(t / (1.f + __expf(-t)));
    float f0[4] = {0.f, 0.f, 0.f, 0.f}, f1[4] = {0.f, 0.f, 0.f, 0.f};
#define FFN2_STEP(J)                                                                  \
    {                                                                                 \
      const float h0 = dpp<BCAST + (J)>(hval), h1 = dpp<BCAST + 8 + (J)>(hval);       \
      const float w0 = bflo(w2r[J][0]), w1_ = bfhi(w2r[J][0]), w2_ = bflo(w2r[J][1]), w3 = bfhi(w2r[J][1]); \
      f0[0] = fmaf(h0, w0, f0[0]); f0[1] = fmaf(h0, w1_, f0[1]); f0[2] = fmaf(h0, w2_, f0[2]); f0[3] = fmaf(h0, w3, f0[3]); \
      f1[0] = fmaf(h1, w0, f1[0]); f1[1] = fmaf(h1, w1_, f1[1]); f1[2] = fmaf(h1, w2_, f1[2]); f1[3] = fmaf(h1, w3, f1[3]); \
    }
    FFN2_STEP(0) FFN2_STEP(1) FFN2_STEP(2) FFN2_STEP(3) FFN2_STEP(4) FFN2_STEP(5) FFN2_STEP(6) FFN2_STEP(7)
#undef FFN2_STEP
    asm volatile("" : "+v"(f0[0]), "+v"(f0[1]), "+v"(f0[2]), "+v"(f0[3]), "+v"(f1[0]), "+v"(f1[1]), "+v"(f1[2]), "+v"(f1[3]));
    if (ffn_on && tid < 160) {
      const u32 fo = (u32)(WS_F + (size_t)fg * NB * DM + tid * 4) * 4;
      __builtin_amdgcn_raw_buffer_store_b128(__builtin_bit_cast(u32x4, f32x4{f0[0], f0[1], f0[2], f0[3]}), wsr, fo, 0, SC1);
      __builtin_amdgcn_raw_buffer_store_b128(__builtin_bit_cast(u32x4, f32x4{f1[0], f1[1], f1[2], f1[3]}), wsr, fo + DM * 4, 0, SC1);
    }
  }
  SCHED_FENCE();

  float acc[8];
#pragma unroll
  for (int k = 0; k < 8; ++k) acc[k] = 0.f;
#define PV_STEP(I)                                                                    \
  {                                                                                   \
    const float pb = dpp<BCAST + ((I) & 15)>(p[(I) >> 4]);                            \
    const u32x4 vv = KV[32 + (I)];                                                    \
    _Pragma("unroll") for (int k = 0; k < 4; ++k) {                                   \
      acc[2 * k] = fmaf(pb, bflo(vv[k]), acc[2 * k]);                                 \
      acc[2 * k + 1] = fmaf(pb, bfhi(vv[k]), acc[2 * k + 1]);                         \
    }                                                                                 \
    AFTER_KV(32 + (I));                                                               \
  }
  PV_STEP(0) PV_STEP(1) PV_STEP(2) PV_STEP(3) PV_STEP(4) PV_STEP(5) PV_STEP(6) PV_STEP(7)
  PV_STEP(8) PV_STEP(9) PV_STEP(10) PV_STEP(11) PV_STEP(12) PV_STEP(13) PV_STEP(14) PV_STEP(15)
  PV_STEP(16) PV_STEP(17) PV_STEP(18) PV_STEP(19) PV_STEP(20) PV_STEP(21) PV_STEP(22) PV_STEP(23)
  PV_STEP(24) PV_STEP(25) PV_STEP(26) PV_STEP(27) PV_STEP(28) PV_STEP(29) PV_STEP(30) PV_STEP(31)
#undef PV_STEP
  STAMP(6);
#pragma unroll
  for (int k = 0; k < 8; ++k) acc[k] = xrow_sum(acc[k]);
  if (r == 0) {
    *reinterpret_cast<f32x4*>(&lds[L_OP + wave * DH + 8 * c]) = f32x4{acc[0], acc[1], acc[2], acc[3]};
    *reinterpret_cast<f32x4*>(&lds[L_OP + wave * DH + 8 * c + 4]) = f32x4{acc[4], acc[5], acc[6], acc[7]};
  }
  __syncthreads();

  STAMP(7);
  {
    f32x4 o4 = {0.f, 0.f, 0.f, 0.f};
#pragma unroll
    for (int w = 0; w < 8; ++w) o4 += *reinterpret_cast<const f32x4*>(&lds[L_OP + w * DH + (wave * 4 + r) * 4]);
    if (tid == 0) {
      const f32x4 t0 = *reinterpret_cast<const f32x4*>(&lds[L_ML + 8]), t1 = *reinterpret_cast<const f32x4*>(&lds[L_ML + 12]);
      const float L = ((t0[0] + t0[1]) + (t0[2] + t0[3])) + ((t1[0] + t1[1]) + (t1[2] + t1[3]));
      __builtin_amdgcn_raw_buffer_store_b64(__builtin_bit_cast(u32x2, f32x2{m2, L}), wsr, (u32)(WS_ML + (size_t)((b * NH + h) * NSPLIT + s) * 2) * 4, 0, SC1);
    }
    const u32 o01 = pk_bf(o4[0], o4[1]), o23 = pk_bf(o4[2], o4[3]);
    float y[40];
#pragma unroll
    for (int seg = 0; seg < 5; ++seg)
#pragma unroll
      for (int k = 0; k < 4; ++k) {
        y[(2 * k) * 5 + seg] = dot2(Wo[2 * (5 + seg)][k], o23, dot2(Wo[2 * seg][k], o01, 0.f));
        y[(2 * k + 1) * 5 + seg] = dot2(Wo[2 * (5 + seg) + 1][k], o23, dot2(Wo[2 * seg + 1][k], o01, 0.f));
      }
    float z[20];
#pragma unroll
    for (int v = 0; v < 20; ++v) {
      auto t = __builtin_amdgcn_permlane32_swap(__float_as_uint(y[v]), __float_as_uint(y[v + 20]), false, false);
      z[v] = __uint_as_float(t[0]) + __uint_as_float(t[1]);
    }
    float u[10];
#pragma unroll
    for (int v = 0; v < 10; ++v) {
      auto t = __builtin_amdgcn_permlane16_swap(__float_as_uint(z[v]), __float_as_uint(z[v + 10]), false, false);
      u[v] = __uint_as_float(t[0]) + __uint_as_float(t[1]);
    }
#pragma unroll
    for (int seg = 0; seg < 5; ++seg)
      *reinterpret_cast<f32x2*>(&lds[L_RED + wave * DM + seg * 128 + 8 * c + 2 * r]) = f32x2{u[seg], u[5 + seg]};
  }
  __syncthreads();
  if (tid < 160) {
    f32x4 t = {0.f, 0.f, 0.f, 0.f};
#pragma unroll
    for (int w = 0; w < NWAVE; ++w) t += *reinterpret_cast<const f32x4*>(&lds[L_RED + w * DM + tid * 4]);
    __builtin_amdgcn_raw_buffer_store_b128(__builtin_bit_cast(u32x4, t), wsr, (u32)(WS_P + (size_t)((b * NH + h) * NSPLIT + s) * DM + tid * 4) * 4, 0, SC1);
  }
  asm volatile("s_waitcnt vmcnt(0)" ::: "memory");
  __syncthreads();
  const unsigned long long tag = epoch + 1ull;
  if (tid == 0) __hip_atomic_store((gu64*)(ws + WS_FLAG) + bid, tag, RLX_AGENT);
  STAMP(8);

  if (bid < NB * 20) {
    const int rb = bid / 20, cb = bid % 20;
    u32 xg[5], gg[5];
#pragma unroll
    for (int j = 0; j < 5; ++j) {
      xg[j] = *reinterpret_cast<const u32*>(x + rb * DM + 2 * (lane + 64 * j));
      gg[j] = *reinterpret_cast<const u32*>(gw + 2 * (lane + 64 * j));
    }
    if (wave == 0) {
      gu64* fl = (gu64*)(ws + WS_FLAG) + 4 * lane;
      for (unsigned spins = 0; spins < (1u << 22); ++spins) {
        const unsigned long long f0 = __hip_atomic_load(fl, RLX_AGENT), f1 = __hip_atomic_load(fl + 1, RLX_AGENT);
        const unsigned long long f2 = __hip_atomic_load(fl + 2, RLX_AGENT), f3 = __hip_atomic_load(fl + 3, RLX_AGENT);
        if (__all(f0 == tag && f1 == tag && f2 == tag && f3 == tag)) break;
        __builtin_amdgcn_s_sleep(1);
      }
      if (bid == 0 && lane == 0) __hip_atomic_store((gu64*)(ws + WS_EPOCH), tag, RLX_AGENT);
    }
    asm volatile("" ::: "memory");
    __syncthreads();
    const int c4 = tid & 7, rg = tid >> 3;
    const int col = cb * 32 + c4 * 4;
    const int hh = rg >> 1, s0 = (rg & 1) * 2;
    const u32 po = (u32)(WS_P + (size_t)((rb * NH + hh) * NSPLIT + s0) * DM + col) * 4;
    const f32x4 pr0 = __builtin_bit_cast(f32x4, __builtin_amdgcn_raw_buffer_load_b128(wsr, po, 0, SC1));
    const f32x4 pr1 = __builtin_bit_cast(f32x4, __builtin_amdgcn_raw_buffer_load_b128(wsr, po + DM * 4, 0, SC1));
    const u32 mo = (u32)(WS_ML + (size_t)(rb * NH + hh) * NSPLIT * 2) * 4;
    const f32x4 ml0 = __builtin_bit_cast(f32x4, __builtin_amdgcn_raw_buffer_load_b128(wsr, mo, 0, SC1));
    const f32x4 ml1 = __builtin_bit_cast(f32x4, __builtin_amdgcn_raw_buffer_load_b128(wsr, mo + 16, 0, SC1));
    f32x4 fr[4];
#pragma unroll
    for (int j = 0; j < 4; ++j) {
      const int f = rg + 64 * j;
      const int fc = f < NFG ? f : NFG - 1;
      fr[j] = __builtin_bit_cast(f32x4, __builtin_amdgcn_raw_buffer_load_b128(wsr, (u32)(WS_F + (size_t)(fc * NB + rb) * DM + col) * 4, 0, SC1));
    }
    float gs = 0.f;
#pragma unroll
    for (int j = 0; j < 5; ++j) gs += rbf(bflo(xg[j]) * bflo(gg[j])) + rbf(bfhi(xg[j]) * bfhi(gg[j]));
    gs = rbf(xrow_sum(row_sum(gs)));
    const float g = rbf(1.f / (1.f + __expf(-gs)));
    const float M = fmaxf(fmaxf(ml0[0], ml0[2]), fmaxf(ml1[0], ml1[2]));
    const float e0 = __builtin_amdgcn_exp2f(ml0[0] - M), e1 = __builtin_amdgcn_exp2f(ml0[2] - M);
    const float e2 = __builtin_amdgcn_exp2f(ml1[0] - M), e3 = __builtin_amdgcn_exp2f(ml1[2] - M);
    const float inv = 1.f / (e0 * ml0[1] + e1 * ml0[3] + e2 * ml1[1] + e3 * ml1[3]);
    const float wa = (s0 ? e2 : e0) * inv, wb = (s0 ? e3 : e1) * inv;
    f32x4 a = wa * pr0 + wb * pr1;
    f32x4 f = fr[0] + fr[1] + fr[2];
    if (rg + 192 < NFG) f += fr[3];
#pragma unroll
    for (int k = 0; k < 4; ++k) {
      a[k] += dpp<XOR8>(a[k]); a[k] = xrow_sum(a[k]);
      f[k] += dpp<XOR8>(f[k]); f[k] = xrow_sum(f[k]);
    }
    if (lane < 8) {
      *reinterpret_cast<f32x4*>(&lds[wave * 64 + c4 * 4]) = a;
      *reinterpret_cast<f32x4*>(&lds[wave * 64 + 32 + c4 * 4]) = f;
    }
    __syncthreads();
    if (tid < 32) {
      float av = 0.f, fv = 0.f;
#pragma unroll
      for (int w = 0; w < NWAVE; ++w) { av += lds[w * 64 + tid]; fv += lds[w * 64 + 32 + tid]; }
      av = rbf(av); fv = rbf(fv);
      const float t1 = rbf(g * av), t2 = rbf(rbf(1.f - g) * fv);
      outp[rb * DM + cb * 32 + tid] = __builtin_bit_cast(u16, __float2bfloat16(t1 + t2));
    }
  }
}

}

extern "C" void kernel_launch(void* const* d_in, const int* in_sizes, int n_in, void* d_out, int out_size, void* d_ws,
                              size_t ws_size, hipStream_t stream) {
  const u16* x = (const u16*)d_in[0];
  const u16* kc = (const u16*)d_in[1];
  const u16* vc = (const u16*)d_in[2];
  const u16* wq = (const u16*)d_in[3];
  const u16* wo = (const u16*)d_in[4];
  const u16* w1 = (const u16*)d_in[6];
  const u16* w2 = (const u16*)d_in[7];
  const u16* gw = (const u16*)d_in[8];
  float* ws = (float*)d_ws;
  k_main<<<dim3(NB * NH * NSPLIT), dim3(512), 0, stream>>>(x, kc, vc, wq, wo, w1, w2, ws, gw, (u16*)d_out);
}
```

```cpp
#include <hip/hip_runtime.h>
#include <hip/hip_bf16.h>
#include <stdint.h>

typedef unsigned int u32;
typedef unsigned short u16;
typedef __attribute__((ext_vector_type(4))) unsigned u32x4;
typedef __attribute__((ext_vector_type(2))) unsigned u32x2;
typedef __attribute__((ext_vector_type(4))) float f32x4;
typedef __attribute__((ext_vector_type(2))) float f32x2;
typedef __attribute__((ext_vector_type(2))) __bf16 bf16x2;

namespace {

constexpr int NB = 2, DM = 640, NH = 32, SK = 4096, DH = 128, FF = 1728;
constexpr int NSPLIT = 4, TOKWG = SK / NSPLIT, NWAVE = 8, TOKW = TOKWG / NWAVE;
constexpr int NFG = FF / 8;
constexpr float LOG2E = 1.4426950408889634f;
constexpr float SCALE = 0.088388347648318440f;

constexpr size_t WS_EPOCH = 0, WS_FLAG = 64;
constexpr size_t WS_P = WS_FLAG + 512;
constexpr size_t WS_ML = WS_P + (size_t)NB * NH * NSPLIT * DM;
constexpr size_t WS_F = WS_ML + (size_t)NB * NH * NSPLIT * 2;
constexpr size_t WS_END = WS_F + (size_t)NFG * NB * DM;
typedef __attribute__((address_space(1))) unsigned long long gu64;
#define RLX_AGENT __ATOMIC_RELAXED, __HIP_MEMORY_SCOPE_AGENT
#define PSLEEP 4
constexpr int SC1 = 16;

__device__ __forceinline__ float dot2(u32 a, u32 b, float acc) {
  return __builtin_amdgcn_fdot2_f32_bf16(__builtin_bit_cast(bf16x2, a), __builtin_bit_cast(bf16x2, b), acc, false);
}
__device__ __forceinline__ float bflo(u32 v) { return __builtin_bit_cast(float, v << 16); }
__device__ __forceinline__ float bfhi(u32 v) { return __builtin_bit_cast(float, v & 0xffff0000u); }
__device__ __forceinline__ float rbf(float x) { return __bfloat162float(__float2bfloat16(x)); }
__device__ __forceinline__ u32 pk_bf(float lo, float hi) {
  const u32 a = __builtin_bit_cast(u16, __float2bfloat16(lo));
  const u32 b = __builtin_bit_cast(u16, __float2bfloat16(hi));
  return a | (b << 16);
}
template <int CTRL> __device__ __forceinline__ float dpp(float x) {
  return __builtin_bit_cast(float, __builtin_amdgcn_mov_dpp(__builtin_bit_cast(int, x), CTRL, 0xf, 0xf, true));
}
constexpr int XOR1 = 0xB1, XOR2 = 0x4E, XOR7 = 0x141, XOR8 = 0x128, BCAST = 0x150;

__device__ __forceinline__ float xrow_sum(float x) {
  auto s = __builtin_amdgcn_permlane16_swap(__float_as_uint(x), __float_as_uint(x), false, false);
  x = __uint_as_float(s[0]) + __uint_as_float(s[1]);
  auto t = __builtin_amdgcn_permlane32_swap(__float_as_uint(x), __float_as_uint(x), false, false);
  return __uint_as_float(t[0]) + __uint_as_float(t[1]);
}
__device__ __forceinline__ float xrow_max(float x) {
  auto s = __builtin_amdgcn_permlane16_swap(__float_as_uint(x), __float_as_uint(x), false, false);
  x = fmaxf(__uint_as_float(s[0]), __uint_as_float(s[1]));
  auto t = __builtin_amdgcn_permlane32_swap(__float_as_uint(x), __float_as_uint(x), false, false);
  return fmaxf(__uint_as_float(t[0]), __uint_as_float(t[1]));
}
__device__ __forceinline__ float row_sum(float x) {
  x += dpp<XOR1>(x); x += dpp<XOR2>(x); x += dpp<XOR7>(x); x += dpp<XOR8>(x); return x;
}
__device__ __forceinline__ float row_max(float x) {
  x = fmaxf(x, dpp<XOR1>(x)); x = fmaxf(x, dpp<XOR2>(x)); x = fmaxf(x, dpp<XOR7>(x)); x = fmaxf(x, dpp<XOR8>(x)); return x;
}
__device__ __forceinline__ float tr_reduce16(float (&x)[16], int c) {
#define TR_STEP(HALF, CTRL, BIT)                                                     \
  _Pragma("unroll") for (int h = 0; h < (HALF); ++h) {                               \
    const float lo_ = x[h], hi_ = x[h + (HALF)];                                     \
    const bool up_ = (c & (BIT)) != 0;                                               \
    const float send_ = up_ ? lo_ : hi_, keep_ = up_ ? hi_ : lo_;                    \
    x[h] = keep_ + dpp<CTRL>(send_);                                                 \
  }
  TR_STEP(8, XOR8, 8) TR_STEP(4, XOR7, 4) TR_STEP(2, XOR2, 2) TR_STEP(1, XOR1, 1)
#undef TR_STEP
  return x[0];
}

#define SCHED_FENCE() __builtin_amdgcn_sched_barrier(0)
#define STAMP(i) do {} while (0)

#define KV_NT 1
__device__ __forceinline__ u32x4 ld_nt(const u16* ub, u32 off) {
  return __builtin_nontemporal_load(reinterpret_cast<const u32x4*>(reinterpret_cast<const char*>(ub) + off));
}
#define W_NT 0
#define KV_PAT 1
__device__ __forceinline__ u32x4 ld16(const u16* ub, u32 off) {
  return *reinterpret_cast<const u32x4*>(reinterpret_cast<const char*>(ub) + off);
}
__device__ __forceinline__ u32x2 ld8(const u16* ub, u32 off) {
  return *reinterpret_cast<const u32x2*>(reinterpret_cast<const char*>(ub) + off);
}
__device__ __forceinline__ u32 ld2(const u16* ub, u32 off) {
  return *reinterpret_cast<const u16*>(reinterpret_cast<const char*>(ub) + off);
}

constexpr int L_QP = 0;
constexpr int L_F1 = L_QP + NWAVE * DH;
constexpr int L_OP = L_F1 + NWAVE * 16;
constexpr int L_ML = L_OP + NWAVE * DH;
constexpr int L_RED = L_ML + NWAVE * 2;
constexpr int L_END = L_RED + NWAVE * DM;

__global__ void __launch_bounds__(512, 2)
k_main(const u16* __restrict__ x, const u16* __restrict__ kc, const u16* __restrict__ vc, const u16* __restrict__ wq,
       const u16* __restrict__ wo, const u16* __restrict__ w1, const u16* __restrict__ w2, float* __restrict__ ws,
       const u16* __restrict__ gw, u16* __restrict__ outp) {
  __shared__ __attribute__((aligned(16))) float lds[L_END];
  const int bid = blockIdx.x, xcd = bid & 7, idx = bid >> 3;
  const int h = xcd * 4 + (idx >> 3), b = (idx >> 2) & 1, s = idx & 3;
  const int tid = threadIdx.x, lane = tid & 63, wave = __builtin_amdgcn_readfirstlane(tid >> 6);
  const int r = lane >> 4, c = lane & 15;
  const bool ffn_on = h < 27;
  const int fg = (ffn_on ? h : 26) * 8 + (idx & 7);

  STAMP(0);
  const __amdgpu_buffer_rsrc_t wsr = __builtin_amdgcn_make_buffer_rsrc(ws, 0, (int)(WS_END * 4), 0x00020000);
  const unsigned long long epoch = __hip_atomic_load((gu64*)(ws + WS_EPOCH), RLX_AGENT);
  SCHED_FENCE();
  u32x2 xq[5];
#pragma unroll
  for (int j = 0; j < 5; ++j) xq[j] = ld8(x + b * DM + wave * 80 + 4 * j, r * 40);
  SCHED_FENCE();
  u32x4 Wq[20], KV[64], Wo[20];
#define KVP 1
  const int KVW = (wave >> 2) * 128 + (wave & 3);
#define KVO(i) ((i) * 4)
  const size_t kvbase = ((size_t)(b * NH + h) * SK + s * TOKWG + KVW * 4) * DH;
  const u16* kp = kc + kvbase;
  const u16* vp = vc + kvbase;
  const u32 loff = lane * 16;
#define LDKV(I) ld_nt(((I) < 32 ? kp + KVO(I) * 4 * DH : vp + KVO((I) - 32) * 4 * DH), loff)
  {
    const u32 off = (u32)(r * 20) * (NH * DH * 2) + 16 * c;
#pragma unroll
    for (int j = 0; j < 20; ++j) {
      Wq[j] = ld16(wq + (size_t)(wave * 80 + j) * (NH * DH) + h * DH, off);
      SCHED_FENCE();
      KV[j] = LDKV(j);
      SCHED_FENCE();
    }
  }
#pragma unroll
  for (int i = 20; i < 24; ++i) { KV[i] = LDKV(i); SCHED_FENCE(); }
  const u16* wop = wo + (size_t)(h * DH + wave * 16) * DM;
  const u32 wooff = (u32)r * (4 * DM * 2) + 16 * c;
#define PRE 5
#define ISSUE_T(T)                                                                                  \
  do {                                                                                              \
    if ((T) >= 0 && (T) < 20) {                                                                     \
      SCHED_FENCE();                                                                                \
      Wo[T] = ld16(wop + (2 * (((T) >> 1) / 5) + ((T) & 1)) * DM + (((T) >> 1) % 5) * 128, wooff);  \
      SCHED_FENCE();                                                                                \
      _Pragma("unroll") for (int t_ = 0; t_ < 2; ++t_) {                                            \
        const int kv_ = 24 + 2 * (T) + t_;                                                          \
        KV[kv_] = LDKV(kv_);                                                                        \
        SCHED_FENCE();                                                                              \
      }                                                                                             \
    }                                                                                               \
  } while (0)
#define PERM_Q(Q)                                                                                   \
  do {                                                                                              \
    _Pragma("unroll") for (int k_ = 0; k_ < 4; ++k_) {                                              \
      const u32 a_ = Wo[2 * (Q)][k_], b_ = Wo[2 * (Q) + 1][k_];                                     \
      Wo[2 * (Q)][k_] = __builtin_amdgcn_perm(b_, a_, 0x05040100u);                                 \
      Wo[2 * (Q) + 1][k_] = __builtin_amdgcn_perm(b_, a_, 0x07060302u);                             \
    }                                                                                               \
  } while (0)
#define AFTER_KV(I)                                                                                 \
  do {                                                                                              \
    if ((I) % 3 == 2) ISSUE_T(PRE + (I) / 3);                                                       \
    if ((I) >= 26 && ((I) - 26) % 4 == 0) PERM_Q(((I) - 26) / 4);                                   \
  } while (0)

#define ABAR 0
  STAMP(1);
  {
    float qa[8];
#pragma unroll
    for (int k = 0; k < 8; ++k) qa[k] = 0.f;
#pragma unroll
    for (int j = 0; j < 20; ++j) {
      const u32 xw = xq[j >> 2][(j >> 1) & 1];
      const float xv = (j & 1) ? bfhi(xw) : bflo(xw);
#pragma unroll
      for (int k = 0; k < 4; ++k) {
        qa[2 * k] = fmaf(xv, bflo(Wq[j][k]), qa[2 * k]);
        qa[2 * k + 1] = fmaf(xv, bfhi(Wq[j][k]), qa[2 * k + 1]);
      }
    }
#pragma unroll
    for (int k = 0; k < 8; ++k) qa[k] = xrow_sum(qa[k]);
    if (r == 0) {
      *reinterpret_cast<f32x4*>(&lds[L_QP + wave * DH + 8 * c]) = f32x4{qa[0], qa[1], qa[2], qa[3]};
      *reinterpret_cast<f32x4*>(&lds[L_QP + wave * DH + 8 * c + 4]) = f32x4{qa[4], qa[5], qa[6], qa[7]};
    }
  }
  SCHED_FENCE();
  STAMP(2);
  const u32x4 w1a = ld16(w1 + (size_t)(wave * 80) * FF + fg * 8, (u32)lane * (FF * 2));
  const u32x4 w1b = ld16(w1 + (size_t)(wave * 80 + 64) * FF + fg * 8, (u32)c * (FF * 2));
  const u32 xa0 = ld2(x + wave * 80, 2 * lane), xa1 = ld2(x + DM + wave * 80, 2 * lane);
  const u32 xb0 = ld2(x + wave * 80 + 64, 2 * c), xb1 = ld2(x + DM + wave * 80 + 64, 2 * c);
  SCHED_FENCE();
#pragma unroll
  for (int T = 0; T < PRE; ++T) ISSUE_T(T);
  __syncthreads();
  STAMP(3);
  u32 qpk[4];
  {
    f32x4 q0 = {0.f, 0.f, 0.f, 0.f}, q1 = {0.f, 0.f, 0.f, 0.f};
#pragma unroll
    for (int w = 0; w < NWAVE; ++w) {
      q0 += *reinterpret_cast<const f32x4*>(&lds[L_QP + w * DH + 8 * c]);
      q1 += *reinterpret_cast<const f32x4*>(&lds[L_QP + w * DH + 8 * c + 4]);
    }
    qpk[0] = pk_bf(q0[0], q0[1]); qpk[1] = pk_bf(q0[2], q0[3]);
    qpk[2] = pk_bf(q1[0], q1[1]); qpk[3] = pk_bf(q1[2], q1[3]);
  }

  float sc[2];
  u32x2 w2r[8];
#pragma unroll
  for (int g = 0; g < 2; ++g) {
    float xx[16];
#pragma unroll
    for (int i = 0; i < 16; ++i) {
      const u32x4 kk = KV[g * 16 + i];
      float d = dot2(kk[0], qpk[0], 0.f);
      d = dot2(kk[1], qpk[1], d);
      d = dot2(kk[2], qpk[2], d);
      xx[i] = dot2(kk[3], qpk[3], d);
      AFTER_KV(g * 16 + i);
    }
    STAMP(4 + g);
    const float t = tr_reduce16(xx, c);
    sc[g] = rbf(rbf(t) * SCALE);
    if (g == 0) {
      SCHED_FENCE();
      float fa[16];
      const float a0 = bflo(xa0), a1 = bflo(xa1);
      const float b0 = lane < 16 ? bflo(xb0) : 0.f, b1 = lane < 16 ? bflo(xb1) : 0.f;
#pragma unroll
      for (int k = 0; k < 4; ++k) {
        fa[2 * k] = fmaf(a0, bflo(w1a[k]), b0 * bflo(w1b[k]));
        fa[2 * k + 1] = fmaf(a0, bfhi(w1a[k]), b0 * bfhi(w1b[k]));
        fa[8 + 2 * k] = fmaf(a1, bflo(w1a[k]), b1 * bflo(w1b[k]));
        fa[8 + 2 * k + 1] = fmaf(a1, bfhi(w1a[k]), b1 * bfhi(w1b[k]));
      }
      float ft = tr_reduce16(fa, c);
      ft = xrow_sum(ft);
      if (r == 0) lds[L_F1 + wave * 16 + c] = ft;
      SCHED_FENCE();
      const u32 col = (tid < 160 ? tid : 0) * 8;
#pragma unroll
      for (int j = 0; j < 8; ++j) w2r[j] = ld8(w2 + (size_t)(fg * 8 + j) * DM, col);
      SCHED_FENCE();
    }
  }
  {
    const float mw = xrow_max(row_max(fmaxf(sc[0], sc[1])));
    if (lane == 0) lds[L_ML + wave] = mw;
  }
  __syncthreads();
  float m2;
  {
    const f32x4 t0 = *reinterpret_cast<const f32x4*>(&lds[L_ML]), t1 = *reinterpret_cast<const f32x4*>(&lds[L_ML + 4]);
    m2 = fmaxf(fmaxf(fmaxf(t0[0], t0[1]), fmaxf(t0[2], t0[3])), fmaxf(fmaxf(t1[0], t1[1]), fmaxf(t1[2], t1[3]))) * LOG2E;
  }
  float p[2];
  p[0] = __builtin_amdgcn_exp2f(fmaf(sc[0], LOG2E, -m2));
  p[1] = __builtin_amdgcn_exp2f(fmaf(sc[1], LOG2E, -m2));
  const float lsum = xrow_sum(row_sum(p[0] + p[1]));
  if (lane == 0) lds[L_ML + 8 + wave] = lsum;

  {
    float hval;
    float t = 0.f;
#pragma unroll
    for (int w = 0; w < NWAVE; ++w) t += lds[L_F1 + w * 16 + c];
    t = rbf(t);
    hval = rbf(t / (1.f + __expf(-t)));
    float f0[4] = {0.f, 0.f, 0.f, 0.f}, f1[4] = {0.f, 0.f, 0.f, 0.f};
#define FFN2_STEP(J)                                                                  \
    {                                                                                 \
      const float h0 = dpp<BCAST + (J)>(hval), h1 = dpp<BCAST + 8 + (J)>(hval);       \
      const float w0 = bflo(w2r[J][0]), w1_ = bfhi(w2r[J][0]), w2_ = bflo(w2r[J][1]), w3 = bfhi(w2r[J][1]); \
      f0[0] = fmaf(h0, w0, f0[0]); f0[1] = fmaf(h0, w1_, f0[1]); f0[2] = fmaf(h0, w2_, f0[2]); f0[3] = fmaf(h0, w3, f0[3]); \
      f1[0] = fmaf(h1, w0, f1[0]); f1[1] = fmaf(h1, w1_, f1[1]); f1[2] = fmaf(h1, w2_, f1[2]); f1[3] = fmaf(h1, w3, f1[3]); \
    }
    FFN2_STEP(0) FFN2_STEP(1) FFN2_STEP(2) FFN2_STEP(3) FFN2_STEP(4) FFN2_STEP(5) FFN2_STEP(6) FFN2_STEP(7)
#undef FFN2_STEP
    asm volatile("" : "+v"(f0[0]), "+v"(f0[1]), "+v"(f0[2]), "+v"(f0[3]), "+v"(f1[0]), "+v"(f1[1]), "+v"(f1[2]), "+v"(f1[3]));
    if (ffn_on && tid < 160) {
      const u32 fo = (u32)(WS_F + (size_t)fg * NB * DM + tid * 4) * 4;
      __builtin_amdgcn_raw_buffer_store_b128(__builtin_bit_cast(u32x4, f32x4{f0[0], f0[1], f0[2], f0[3]}), wsr, fo, 0, SC1);
      __builtin_amdgcn_raw_buffer_store_b128(__builtin_bit_cast(u32x4, f32x4{f1[0], f1[1], f1[2], f1[3]}), wsr, fo + DM * 4, 0, SC1);
    }
  }
  SCHED_FENCE();

  float acc[8];
#pragma unroll
  for (int k = 0; k < 8; ++k) acc[k] = 0.f;
#define PV_STEP(I)                                                                    \
  {                                                                                   \
    const float pb = dpp<BCAST + ((I) & 15)>(p[(I) >> 4]);                            \
    const u32x4 vv = KV[32 + (I)];                                                    \
    _Pragma("unroll") for (int k = 0; k < 4; ++k) {                                   \
      acc[2 * k] = fmaf(pb, bflo(vv[k]), acc[2 * k]);                                 \
      acc[2 * k + 1] = fmaf(pb, bfhi(vv[k]), acc[2 * k + 1]);                         \
    }                                                                                 \
    AFTER_KV(32 + (I));                                                               \
  }
  PV_STEP(0) PV_STEP(1) PV_STEP(2) PV_STEP(3) PV_STEP(4) PV_STEP(5) PV_STEP(6) PV_STEP(7)
  PV_STEP(8) PV_STEP(9) PV_STEP(10) PV_STEP(11) PV_STEP(12) PV_STEP(13) PV_STEP(14) PV_STEP(15)
  PV_STEP(16) PV_STEP(17) PV_STEP(18) PV_STEP(19) PV_STEP(20) PV_STEP(21) PV_STEP(22) PV_STEP(23)
  PV_STEP(24) PV_STEP(25) PV_STEP(26) PV_STEP(27) PV_STEP(28) PV_STEP(29) PV_STEP(30) PV_STEP(31)
#undef PV_STEP
  STAMP(6);
#pragma unroll
  for (int k = 0; k < 8; ++k) acc[k] = xrow_sum(acc[k]);
  if (r == 0) {
    *reinterpret_cast<f32x4*>(&lds[L_OP + wave * DH + 8 * c]) = f32x4{acc[0], acc[1], acc[2], acc[3]};
    *reinterpret_cast<f32x4*>(&lds[L_OP + wave * DH + 8 * c + 4]) = f32x4{acc[4], acc[5], acc[6], acc[7]};
  }
  __syncthreads();

  STAMP(7);
  {
    f32x4 o4 = {0.f, 0.f, 0.f, 0.f};
#pragma unroll
    for (int w = 0; w < 8; ++w) o4 += *reinterpret_cast<const f32x4*>(&lds[L_OP + w * DH + (wave * 4 + r) * 4]);
    if (tid == 0) {
      const f32x4 t0 = *reinterpret_cast<const f32x4*>(&lds[L_ML + 8]), t1 = *reinterpret_cast<const f32x4*>(&lds[L_ML + 12]);
      const float L = ((t0[0] + t0[1]) + (t0[2] + t0[3])) + ((t1[0] + t1[1]) + (t1[2] + t1[3]));
      __builtin_amdgcn_raw_buffer_store_b64(__builtin_bit_cast(u32x2, f32x2{m2, L}), wsr, (u32)(WS_ML + (size_t)((b * NH + h) * NSPLIT + s) * 2) * 4, 0, SC1);
    }
    const u32 o01 = pk_bf(o4[0], o4[1]), o23 = pk_bf(o4[2], o4[3]);
    float y[40];
#pragma unroll
    for (int seg = 0; seg < 5; ++seg)
#pragma unroll
      for (int k = 0; k < 4; ++k) {
        y[(2 * k) * 5 + seg] = dot2(Wo[2 * (5 + seg)][k], o23, dot2(Wo[2 * seg][k], o01, 0.f));
        y[(2 * k + 1) * 5 + seg] = dot2(Wo[2 * (5 + seg) + 1][k], o23, dot2(Wo[2 * seg + 1][k], o01, 0.f));
      }
    float z[20];
#pragma unroll
    for (int v = 0; v < 20; ++v) {
      auto t = __builtin_amdgcn_permlane32_swap(__float_as_uint(y[v]), __float_as_uint(y[v + 20]), false, false);
      z[v] = __uint_as_float(t[0]) + __uint_as_float(t[1]);
    }
    float u[10];
#pragma unroll
    for (int v = 0; v < 10; ++v) {
      auto t = __builtin_amdgcn_permlane16_swap(__float_as_uint(z[v]), __float_as_uint(z[v + 10]), false, false);
      u[v] = __uint_as_float(t[0]) + __uint_as_float(t[1]);
    }
#pragma unroll
    for (int seg = 0; seg < 5; ++seg)
      *reinterpret_cast<f32x2*>(&lds[L_RED + wave * DM + seg * 128 + 8 * c + 2 * r]) = f32x2{u[seg], u[5 + seg]};
  }
  __syncthreads();
  if (tid < 160) {
    f32x4 t = {0.f, 0.f, 0.f, 0.f};
#pragma unroll
    for (int w = 0; w < NWAVE; ++w) t += *reinterpret_cast<const f32x4*>(&lds[L_RED + w * DM + tid * 4]);
    __builtin_amdgcn_raw_buffer_store_b128(__builtin_bit_cast(u32x4, t), wsr, (u32)(WS_P + (size_t)((b * NH + h) * NSPLIT + s) * DM + tid * 4) * 4, 0, SC1);
  }
  asm volatile("s_waitcnt vmcnt(0)" ::: "memory");
  __syncthreads();
  const unsigned long long tag = epoch + 1ull;
  if (tid == 0) __hip_atomic_store((gu64*)(ws + WS_FLAG) + h * 8 + (idx & 7), tag, RLX_AGENT);
  STAMP(8);

  if (bid < NB * 20) {
    const int rb = bid / 20, cb = bid % 20;
    u32 xg[5], gg[5];
#pragma unroll
    for (int j = 0; j < 5; ++j) {
      xg[j] = *reinterpret_cast<const u32*>(x + rb * DM + 2 * (lane + 64 * j));
      gg[j] = *reinterpret_cast<const u32*>(gw + 2 * (lane + 64 * j));
    }
    {
      gu64* fl = (gu64*)(ws + WS_FLAG) + 32 * wave + (lane & 31);
      for (unsigned spins = 0; spins < (1u << 22); ++spins) {
        if (__all(__hip_atomic_load(fl, RLX_AGENT) == tag)) break;
        __builtin_amdgcn_s_sleep(PSLEEP);
      }
    }
    asm volatile("" ::: "memory");
    const int c4 = tid & 7, rg = tid >> 3;
    const int col = cb * 32 + c4 * 4;
    const int hh = rg >> 1, s0 = (rg & 1) * 2;
    const u32 po = (u32)(WS_P + (size_t)((rb * NH + hh) * NSPLIT + s0) * DM + col) * 4;
    const f32x4 pr0 = __builtin_bit_cast(f32x4, __builtin_amdgcn_raw_buffer_load_b128(wsr, po, 0, SC1));
    const f32x4 pr1 = __builtin_bit_cast(f32x4, __builtin_amdgcn_raw_buffer_load_b128(wsr, po + DM * 4, 0, SC1));
    const u32 mo = (u32)(WS_ML + (size_t)(rb * NH + hh) * NSPLIT * 2) * 4;
    const f32x4 ml0 = __builtin_bit_cast(f32x4, __builtin_amdgcn_raw_buffer_load_b128(wsr, mo, 0, SC1));
    const f32x4 ml1 = __builtin_bit_cast(f32x4, __builtin_amdgcn_raw_buffer_load_b128(wsr, mo + 16, 0, SC1));
    f32x4 fr[4];
    const bool fon = hh < 27;
#pragma unroll
    for (int j = 0; j < 4; ++j) {
      const int fc = fon ? 4 * rg + j : 0;
      fr[j] = __builtin_bit_cast(f32x4, __builtin_amdgcn_raw_buffer_load_b128(wsr, (u32)(WS_F + (size_t)(fc * NB + rb) * DM + col) * 4, 0, SC1));
    }
    float gs = 0.f;
#pragma unroll
    for (int j = 0; j < 5; ++j) gs += rbf(bflo(xg[j]) * bflo(gg[j])) + rbf(bfhi(xg[j]) * bfhi(gg[j]));
    gs = rbf(xrow_sum(row_sum(gs)));
    const float g = rbf(1.f / (1.f + __expf(-gs)));
    const float M = fmaxf(fmaxf(ml0[0], ml0[2]), fmaxf(ml1[0], ml1[2]));
    const float e0 = __builtin_amdgcn_exp2f(ml0[0] - M), e1 = __builtin_amdgcn_exp2f(ml0[2] - M);
    const float e2 = __builtin_amdgcn_exp2f(ml1[0] - M), e3 = __builtin_amdgcn_exp2f(ml1[2] - M);
    const float inv = 1.f / (e0 * ml0[1] + e1 * ml0[3] + e2 * ml1[1] + e3 * ml1[3]);
    const float wa = (s0 ? e2 : e0) * inv, wb = (s0 ? e3 : e1) * inv;
    f32x4 a = wa * pr0 + wb * pr1;
    f32x4 f = (fr[0] + fr[1]) + (fr[2] + fr[3]);
    if (!fon) f = f32x4{0.f, 0.f, 0.f, 0.f};
#pragma unroll
    for (int k = 0; k < 4; ++k) {
      a[k] += dpp<XOR8>(a[k]); a[k] = xrow_sum(a[k]);
      f[k] += dpp<XOR8>(f[k]); f[k] = xrow_sum(f[k]);
    }
    if (lane < 8) {
      *reinterpret_cast<f32x4*>(&lds[wave * 64 + c4 * 4]) = a;
      *reinterpret_cast<f32x4*>(&lds[wave * 64 + 32 + c4 * 4]) = f;
    }
    __syncthreads();
    if (bid == 0 && tid == 64) __hip_atomic_store((gu64*)(ws + WS_EPOCH), tag, RLX_AGENT);
    if (tid < 32) {
      float av = 0.f, fv = 0.f;
#pragma unroll
      for (int w = 0; w < NWAVE; ++w) { av += lds[w * 64 + tid]; fv += lds[w * 64 + 32 + tid]; }
      av = rbf(av); fv = rbf(fv);
      const float t1 = rbf(g * av), t2 = rbf(rbf(1.f - g) * fv);
      outp[rb * DM + cb * 32 + tid] = __builtin_bit_cast(u16, __float2bfloat16(t1 + t2));
    }
  }
}

}

extern "C" void kernel_launch(void* const* d_in, const int* in_sizes, int n_in, void* d_out, int out_size, void* d_ws,
                              size_t ws_size, hipStream_t stream) {
  const u16* x = (const u16*)d_in[0];
  const u16* kc = (const u16*)d_in[1];
  const u16* vc = (const u16*)d_in[2];
  const u16* wq = (const u16*)d_in[3];
  const u16* wo = (const u16*)d_in[4];
  const u16* w1 = (const u16*)d_in[6];
  const u16* w2 = (const u16*)d_in[7];
  const u16* gw = (const u16*)d_in[8];
  float* ws = (float*)d_ws;
  k_main<<<dim3(NB * NH * NSPLIT), dim3(512), 0, stream>>>(x, kc, vc, wq, wo, w1, w2, ws, gw, (u16*)d_out);
}
```

```cpp
#include <hip/hip_runtime.h>
#include <hip/hip_bf16.h>
#include <stdint.h>

typedef unsigned int u32;
typedef unsigned short u16;
typedef __attribute__((ext_vector_type(4))) unsigned u32x4;
typedef __attribute__((ext_vector_type(2))) unsigned u32x2;
typedef __attribute__((ext_vector_type(4))) float f32x4;
typedef __attribute__((ext_vector_type(2))) float f32x2;
typedef __attribute__((ext_vector_type(2))) __bf16 bf16x2;

namespace {

constexpr int NB = 2, DM = 640, NH = 32, SK = 4096, DH = 128, FF = 1728;
constexpr int NSPLIT = 4, TOKWG = SK / NSPLIT, NWAVE = 8, TOKW = TOKWG / NWAVE;
constexpr int NFG = FF / 8;
constexpr float LOG2E = 1.4426950408889634f;
constexpr float SCALE = 0.088388347648318440f;

constexpr size_t WS_P = 0;
constexpr size_t WS_ML = WS_P + (size_t)NB * NH * NSPLIT * DM;
constexpr size_t WS_F = WS_ML + (size_t)NB * NH * NSPLIT * 2;

__device__ __forceinline__ float dot2(u32 a, u32 b, float acc) {
  return __builtin_amdgcn_fdot2_f32_bf16(__builtin_bit_cast(bf16x2, a), __builtin_bit_cast(bf16x2, b), acc, false);
}
__device__ __forceinline__ float bflo(u32 v) { return __builtin_bit_cast(float, v << 16); }
__device__ __forceinline__ float bfhi(u32 v) { return __builtin_bit_cast(float, v & 0xffff0000u); }
__device__ __forceinline__ float rbf(float x) { return __bfloat162float(__float2bfloat16(x)); }
__device__ __forceinline__ u32 pk_bf(float lo, float hi) {
  const u32 a = __builtin_bit_cast(u16, __float2bfloat16(lo));
  const u32 b = __builtin_bit_cast(u16, __float2bfloat16(hi));
  return a | (b << 16);
}
template <int CTRL> __device__ __forceinline__ float dpp(float x) {
  return __builtin_bit_cast(float, __builtin_amdgcn_mov_dpp(__builtin_bit_cast(int, x), CTRL, 0xf, 0xf, true));
}
constexpr int XOR1 = 0xB1, XOR2 = 0x4E, XOR7 = 0x141, XOR8 = 0x128, BCAST = 0x150;

__device__ __forceinline__ float xrow_sum(float x) {
  auto s = __builtin_amdgcn_permlane16_swap(__float_as_uint(x), __float_as_uint(x), false, false);
  x = __uint_as_float(s[0]) + __uint_as_float(s[1]);
  auto t = __builtin_amdgcn_permlane32_swap(__float_as_uint(x), __float_as_uint(x), false, false);
  return __uint_as_float(t[0]) + __uint_as_float(t[1]);
}
__device__ __forceinline__ float xrow_max(float x) {
  auto s = __builtin_amdgcn_permlane16_swap(__float_as_uint(x), __float_as_uint(x), false, false);
  x = fmaxf(__uint_as_float(s[0]), __uint_as_float(s[1]));
  auto t = __builtin_amdgcn_permlane32_swap(__float_as_uint(x), __float_as_uint(x), false, false);
  return fmaxf(__uint_as_float(t[0]), __uint_as_float(t[1]));
}
__device__ __forceinline__ float row_sum(float x) {
  x += dpp<XOR1>(x); x += dpp<XOR2>(x); x += dpp<XOR7>(x); x += dpp<XOR8>(x); return x;
}
__device__ __forceinline__ float row_max(float x) {
  x = fmaxf(x, dpp<XOR1>(x)); x = fmaxf(x, dpp<XOR2>(x)); x = fmaxf(x, dpp<XOR7>(x)); x = fmaxf(x, dpp<XOR8>(x)); return x;
}
__device__ __forceinline__ float tr_reduce16(float (&x)[16], int c) {
#define TR_STEP(HALF, CTRL, BIT)                                                     \
  _Pragma("unroll") for (int h = 0; h < (HALF); ++h) {                               \
    const float lo_ = x[h], hi_ = x[h + (HALF)];                                     \
    const bool up_ = (c & (BIT)) != 0;                                               \
    const float send_ = up_ ? lo_ : hi_, keep_ = up_ ? hi_ : lo_;                    \
    x[h] = keep_ + dpp<CTRL>(send_);                                                 \
  }
  TR_STEP(8, XOR8, 8) TR_STEP(4, XOR7, 4) TR_STEP(2, XOR2, 2) TR_STEP(1, XOR1, 1)
#undef TR_STEP
  return x[0];
}

#define SCHED_FENCE() __builtin_amdgcn_sched_barrier(0)
#define STAMP(i) do {} while (0)

#define KV_NT 1
__device__ __forceinline__ u32x4 ld_nt(const u16* ub, u32 off) {
  return __builtin_nontemporal_load(reinterpret_cast<const u32x4*>(reinterpret_cast<const char*>(ub) + off));
}
#define W_NT 0
#define KV_PAT 1
__device__ __forceinline__ u32x4 ld16(const u16* ub, u32 off) {
  return *reinterpret_cast<const u32x4*>(reinterpret_cast<const char*>(ub) + off);
}
__device__ __forceinline__ u32x2 ld8(const u16* ub, u32 off) {
  return *reinterpret_cast<const u32x2*>(reinterpret_cast<const char*>(ub) + off);
}
__device__ __forceinline__ u32 ld2(const u16* ub, u32 off) {
  return *reinterpret_cast<const u16*>(reinterpret_cast<const char*>(ub) + off);
}

constexpr int L_QP = 0;
constexpr int L_F1 = L_QP + NWAVE * DH;
constexpr int L_OP = L_F1 + NWAVE * 16;
constexpr int L_ML = L_OP + NWAVE * DH;
constexpr int L_RED = L_ML + NWAVE * 2;
constexpr int L_END = L_RED + NWAVE * DM;

__global__ void __launch_bounds__(512, 2)
k_main(const u16* __restrict__ x, const u16* __restrict__ kc, const u16* __restrict__ vc, const u16* __restrict__ wq,
       const u16* __restrict__ wo, const u16* __restrict__ w1, const u16* __restrict__ w2, float* __restrict__ ws,
       const u16* __restrict__ gw, const u16* __restrict__ outp) {
  __shared__ __attribute__((aligned(16))) float lds[L_END];
  const int bid = blockIdx.x, xcd = bid & 7, idx = bid >> 3;
  const int h = xcd * 4 + (idx >> 3), b = (idx >> 2) & 1, s = idx & 3;
  const int tid = threadIdx.x, lane = tid & 63, wave = __builtin_amdgcn_readfirstlane(tid >> 6);
  const int r = lane >> 4, c = lane & 15;
  const bool ffn_on = idx < 27;
  const int fg = xcd * 27 + (ffn_on ? idx : 26);

  STAMP(0);
#define TLBPF 0
  u32x2 xq[5];
#pragma unroll
  for (int j = 0; j < 5; ++j) xq[j] = ld8(x + b * DM + wave * 80 + 4 * j, r * 40);
  SCHED_FENCE();
  u32x4 Wq[20], KV[64], Wo[20];
  constexpr int KVSTR = 16 * DH;
  const size_t kvbase = ((size_t)(b * NH + h) * SK + s * TOKWG + (wave >> 2) * (4 * TOKW) + (wave & 3) * 4) * DH;
  const u16* kp = kc + kvbase;
  const u16* vp = vc + kvbase;
  const u32 loff = lane * 16;
  {
    const u32 off = (u32)(r * 20) * (NH * DH * 2) + 16 * c;
#pragma unroll
    for (int j = 0; j < 20; ++j) {
      Wq[j] = ld16(wq + (size_t)(wave * 80 + j) * (NH * DH) + h * DH, off);
      SCHED_FENCE();
      KV[j] = ld_nt(kp + j * KVSTR, loff);
      SCHED_FENCE();
    }
  }
#pragma unroll
  for (int i = 20; i < 24; ++i) { KV[i] = ld_nt(kp + i * KVSTR, loff); SCHED_FENCE(); }
  const u16* wop = wo + (size_t)(h * DH + wave * 16) * DM;
  const u32 wooff = (u32)r * (4 * DM * 2) + 16 * c;
#define PRE 5
#define ISSUE_T(T)                                                                                  \
  do {                                                                                              \
    if ((T) >= 0 && (T) < 20) {                                                                     \
      SCHED_FENCE();                                                                                \
      Wo[T] = ld16(wop + (2 * (((T) >> 1) / 5) + ((T) & 1)) * DM + (((T) >> 1) % 5) * 128, wooff);  \
      SCHED_FENCE();                                                                                \
      _Pragma("unroll") for (int t_ = 0; t_ < 2; ++t_) {                                            \
        const int kv_ = 24 + 2 * (T) + t_;                                                          \
        KV[kv_] = ld_nt((kv_ < 32 ? kp + kv_ * KVSTR : vp + (kv_ - 32) * KVSTR), loff);             \
        SCHED_FENCE();                                                                              \
      }                                                                                             \
    }                                                                                               \
  } while (0)
#define PERM_Q(Q)                                                                                   \
  do {                                                                                              \
    _Pragma("unroll") for (int k_ = 0; k_ < 4; ++k_) {                                              \
      const u32 a_ = Wo[2 * (Q)][k_], b_ = Wo[2 * (Q) + 1][k_];                                     \
      Wo[2 * (Q)][k_] = __builtin_amdgcn_perm(b_, a_, 0x05040100u);                                 \
      Wo[2 * (Q) + 1][k_] = __builtin_amdgcn_perm(b_, a_, 0x07060302u);                             \
    }                                                                                               \
  } while (0)
#define AFTER_KV(I)                                                                                 \
  do {                                                                                              \
    if ((I) % 3 == 2) ISSUE_T(PRE + (I) / 3);                                                       \
    if ((I) >= 26 && ((I) - 26) % 4 == 0) PERM_Q(((I) - 26) / 4);                                   \
  } while (0)

#define ABAR 0
  STAMP(1);
  {
    float qa[8];
#pragma unroll
    for (int k = 0; k < 8; ++k) qa[k] = 0.f;
#pragma unroll
    for (int j = 0; j < 20; ++j) {
      const u32 xw = xq[j >> 2][(j >> 1) & 1];
      const float xv = (j & 1) ? bfhi(xw) : bflo(xw);
#pragma unroll
      for (int k = 0; k < 4; ++k) {
        qa[2 * k] = fmaf(xv, bflo(Wq[j][k]), qa[2 * k]);
        qa[2 * k + 1] = fmaf(xv, bfhi(Wq[j][k]), qa[2 * k + 1]);
      }
    }
#pragma unroll
    for (int k = 0; k < 8; ++k) qa[k] = xrow_sum(qa[k]);
    if (r == 0) {
      *reinterpret_cast<f32x4*>(&lds[L_QP + wave * DH + 8 * c]) = f32x4{qa[0], qa[1], qa[2], qa[3]};
      *reinterpret_cast<f32x4*>(&lds[L_QP + wave * DH + 8 * c + 4]) = f32x4{qa[4], qa[5], qa[6], qa[7]};
    }
  }
  SCHED_FENCE();
  STAMP(2);
  const u32x4 w1a = ld16(w1 + (size_t)(wave * 80) * FF + fg * 8, (u32)lane * (FF * 2));
  const u32x4 w1b = ld16(w1 + (size_t)(wave * 80 + 64) * FF + fg * 8, (u32)c * (FF * 2));
  const u32 xa0 = ld2(x + wave * 80, 2 * lane), xa1 = ld2(x + DM + wave * 80, 2 * lane);
  const u32 xb0 = ld2(x + wave * 80 + 64, 2 * c), xb1 = ld2(x + DM + wave * 80 + 64, 2 * c);
  SCHED_FENCE();
#pragma unroll
  for (int T = 0; T < PRE; ++T) ISSUE_T(T);
  __syncthreads();
  STAMP(3);
  u32 qpk[4];
  {
    f32x4 q0 = {0.f, 0.f, 0.f, 0.f}, q1 = {0.f, 0.f, 0.f, 0.f};
#pragma unroll
    for (int w = 0; w < NWAVE; ++w) {
      q0 += *reinterpret_cast<const f32x4*>(&lds[L_QP + w * DH + 8 * c]);
      q1 += *reinterpret_cast<const f32x4*>(&lds[L_QP + w * DH + 8 * c + 4]);
    }
    qpk[0] = pk_bf(q0[0], q0[1]); qpk[1] = pk_bf(q0[2], q0[3]);
    qpk[2] = pk_bf(q1[0], q1[1]); qpk[3] = pk_bf(q1[2], q1[3]);
  }

  float sc[2];
  u32x2 w2r[8];
#pragma unroll
  for (int g = 0; g < 2; ++g) {
    float xx[16];
#pragma unroll
    for (int i = 0; i < 16; ++i) {
      const u32x4 kk = KV[g * 16 + i];
      float d = dot2(kk[0], qpk[0], 0.f);
      d = dot2(kk[1], qpk[1], d);
      d = dot2(kk[2], qpk[2], d);
      xx[i] = dot2(kk[3], qpk[3], d);
      AFTER_KV(g * 16 + i);
    }
    STAMP(4 + g);
    const float t = tr_reduce16(xx, c);
    sc[g] = rbf(rbf(t) * SCALE);
    if (g == 0) {
      SCHED_FENCE();
      float fa[16];
      const float a0 = bflo(xa0), a1 = bflo(xa1);
      const float b0 = lane < 16 ? bflo(xb0) : 0.f, b1 = lane < 16 ? bflo(xb1) : 0.f;
#pragma unroll
      for (int k = 0; k < 4; ++k) {
        fa[2 * k] = fmaf(a0, bflo(w1a[k]), b0 * bflo(w1b[k]));
        fa[2 * k + 1] = fmaf(a0, bfhi(w1a[k]), b0 * bfhi(w1b[k]));
        fa[8 + 2 * k] = fmaf(a1, bflo(w1a[k]), b1 * bflo(w1b[k]));
        fa[8 + 2 * k + 1] = fmaf(a1, bfhi(w1a[k]), b1 * bfhi(w1b[k]));
      }
      float ft = tr_reduce16(fa, c);
      ft = xrow_sum(ft);
      if (r == 0) lds[L_F1 + wave * 16 + c] = ft;
      SCHED_FENCE();
      const u32 col = (tid < 160 ? tid : 0) * 8;
#pragma unroll
      for (int j = 0; j < 8; ++j) w2r[j] = ld8(w2 + (size_t)(fg * 8 + j) * DM, col);
      SCHED_FENCE();
    }
  }
  {
    const float mw = xrow_max(row_max(fmaxf(sc[0], sc[1])));
    if (lane == 0) lds[L_ML + wave] = mw;
  }
  __syncthreads();
  float m2;
  {
    const f32x4 t0 = *reinterpret_cast<const f32x4*>(&lds[L_ML]), t1 = *reinterpret_cast<const f32x4*>(&lds[L_ML + 4]);
    m2 = fmaxf(fmaxf(fmaxf(t0[0], t0[1]), fmaxf(t0[2], t0[3])), fmaxf(fmaxf(t1[0], t1[1]), fmaxf(t1[2], t1[3]))) * LOG2E;
  }
  float p[2];
  p[0] = __builtin_amdgcn_exp2f(fmaf(sc[0], LOG2E, -m2));
  p[1] = __builtin_amdgcn_exp2f(fmaf(sc[1], LOG2E, -m2));
  const float lsum = xrow_sum(row_sum(p[0] + p[1]));
  if (lane == 0) lds[L_ML + 8 + wave] = lsum;

  {
    float hval;
    float t = 0.f;
#pragma unroll
    for (int w = 0; w < NWAVE; ++w) t += lds[L_F1 + w * 16 + c];
    t = rbf(t);
    hval = rbf(t / (1.f + __expf(-t)));
    float f0[4] = {0.f, 0.f, 0.f, 0.f}, f1[4] = {0.f, 0.f, 0.f, 0.f};
#define FFN2_STEP(J)                                                                  \
    {                                                                                 \
      const float h0 = dpp<BCAST + (J)>(hval), h1 = dpp<BCAST + 8 + (J)>(hval);       \
      const float w0 = bflo(w2r[J][0]), w1_ = bfhi(w2r[J][0]), w2_ = bflo(w2r[J][1]), w3 = bfhi(w2r[J][1]); \
      f0[0] = fmaf(h0, w0, f0[0]); f0[1] = fmaf(h0, w1_, f0[1]); f0[2] = fmaf(h0, w2_, f0[2]); f0[3] = fmaf(h0, w3, f0[3]); \
      f1[0] = fmaf(h1, w0, f1[0]); f1[1] = fmaf(h1, w1_, f1[1]); f1[2] = fmaf(h1, w2_, f1[2]); f1[3] = fmaf(h1, w3, f1[3]); \
    }
    FFN2_STEP(0) FFN2_STEP(1) FFN2_STEP(2) FFN2_STEP(3) FFN2_STEP(4) FFN2_STEP(5) FFN2_STEP(6) FFN2_STEP(7)
#undef FFN2_STEP
    asm volatile("" : "+v"(f0[0]), "+v"(f0[1]), "+v"(f0[2]), "+v"(f0[3]), "+v"(f1[0]), "+v"(f1[1]), "+v"(f1[2]), "+v"(f1[3]));
    if (ffn_on && tid < 160) {
      float* fp = ws + WS_F + (size_t)fg * NB * DM + tid * 4;
      *reinterpret_cast<f32x4*>(fp) = f32x4{f0[0], f0[1], f0[2], f0[3]};
      *reinterpret_cast<f32x4*>(fp + DM) = f32x4{f1[0], f1[1], f1[2], f1[3]};
    }
  }
  SCHED_FENCE();

  float acc[8];
#pragma unroll
  for (int k = 0; k < 8; ++k) acc[k] = 0.f;
#define PV_STEP(I)                                                                    \
  {                                                                                   \
    const float pb = dpp<BCAST + ((I) & 15)>(p[(I) >> 4]);                            \
    const u32x4 vv = KV[32 + (I)];                                                    \
    _Pragma("unroll") for (int k = 0; k < 4; ++k) {                                   \
      acc[2 * k] = fmaf(pb, bflo(vv[k]), acc[2 * k]);                                 \
      acc[2 * k + 1] = fmaf(pb, bfhi(vv[k]), acc[2 * k + 1]);                         \
    }                                                                                 \
    AFTER_KV(32 + (I));                                                               \
  }
  PV_STEP(0) PV_STEP(1) PV_STEP(2) PV_STEP(3) PV_STEP(4) PV_STEP(5) PV_STEP(6) PV_STEP(7)
  PV_STEP(8) PV_STEP(9) PV_STEP(10) PV_STEP(11) PV_STEP(12) PV_STEP(13) PV_STEP(14) PV_STEP(15)
  PV_STEP(16) PV_STEP(17) PV_STEP(18) PV_STEP(19) PV_STEP(20) PV_STEP(21) PV_STEP(22) PV_STEP(23)
  PV_STEP(24) PV_STEP(25) PV_STEP(26) PV_STEP(27) PV_STEP(28) PV_STEP(29) PV_STEP(30) PV_STEP(31)
#undef PV_STEP
  STAMP(6);
#pragma unroll
  for (int k = 0; k < 8; ++k) acc[k] = xrow_sum(acc[k]);
  if (r == 0) {
    *reinterpret_cast<f32x4*>(&lds[L_OP + wave * DH + 8 * c]) = f32x4{acc[0], acc[1], acc[2], acc[3]};
    *reinterpret_cast<f32x4*>(&lds[L_OP + wave * DH + 8 * c + 4]) = f32x4{acc[4], acc[5], acc[6], acc[7]};
  }
  __syncthreads();

  STAMP(7);
  {
    f32x4 o4 = {0.f, 0.f, 0.f, 0.f};
#pragma unroll
    for (int w = 0; w < 8; ++w) o4 += *reinterpret_cast<const f32x4*>(&lds[L_OP + w * DH + (wave * 4 + r) * 4]);
    if (tid == 0) {
      const f32x4 t0 = *reinterpret_cast<const f32x4*>(&lds[L_ML + 8]), t1 = *reinterpret_cast<const f32x4*>(&lds[L_ML + 12]);
      const float L = ((t0[0] + t0[1]) + (t0[2] + t0[3])) + ((t1[0] + t1[1]) + (t1[2] + t1[3]));
      *reinterpret_cast<f32x2*>(ws + WS_ML + (size_t)((b * NH + h) * NSPLIT + s) * 2) = f32x2{m2, L};
    }
    const u32 o01 = pk_bf(o4[0], o4[1]), o23 = pk_bf(o4[2], o4[3]);
    float y[40];
#pragma unroll
    for (int seg = 0; seg < 5; ++seg)
#pragma unroll
      for (int k = 0; k < 4; ++k) {
        y[(2 * k) * 5 + seg] = dot2(Wo[2 * (5 + seg)][k], o23, dot2(Wo[2 * seg][k], o01, 0.f));
        y[(2 * k + 1) * 5 + seg] = dot2(Wo[2 * (5 + seg) + 1][k], o23, dot2(Wo[2 * seg + 1][k], o01, 0.f));
      }
    float z[20];
#pragma unroll
    for (int v = 0; v < 20; ++v) {
      auto t = __builtin_amdgcn_permlane32_swap(__float_as_uint(y[v]), __float_as_uint(y[v + 20]), false, false);
      z[v] = __uint_as_float(t[0]) + __uint_as_float(t[1]);
    }
    float u[10];
#pragma unroll
    for (int v = 0; v < 10; ++v) {
      auto t = __builtin_amdgcn_permlane16_swap(__float_as_uint(z[v]), __float_as_uint(z[v + 10]), false, false);
      u[v] = __uint_as_float(t[0]) + __uint_as_float(t[1]);
    }
#pragma unroll
    for (int seg = 0; seg < 5; ++seg)
      *reinterpret_cast<f32x2*>(&lds[L_RED + wave * DM + seg * 128 + 8 * c + 2 * r]) = f32x2{u[seg], u[5 + seg]};
  }
  __syncthreads();
  if (tid < 160) {
    f32x4 t = {0.f, 0.f, 0.f, 0.f};
#pragma unroll
    for (int w = 0; w < NWAVE; ++w) t += *reinterpret_cast<const f32x4*>(&lds[L_RED + w * DM + tid * 4]);
    *reinterpret_cast<f32x4*>(ws + WS_P + (size_t)((b * NH + h) * NSPLIT + s) * DM + tid * 4) = t;
  }
}

__global__ void __launch_bounds__(256)
k_fin(const u16* __restrict__ x, const u16* __restrict__ gw, const float* __restrict__ ws, u16* __restrict__ out) {
  __shared__ __attribute__((aligned(16))) float red[4][2][32];
  const int b = blockIdx.x / 20, cb = blockIdx.x % 20;
  const int tid = threadIdx.x, lane = tid & 63, wave = tid >> 6;
  const int c4 = tid & 7, rg = tid >> 3;
  const int col = cb * 32 + c4 * 4;
  f32x4 pr[4];
#pragma unroll
  for (int s = 0; s < 4; ++s)
    pr[s] = *reinterpret_cast<const f32x4*>(ws + WS_P + (size_t)((b * NH + rg) * NSPLIT + s) * DM + col);
  const f32x4 ml0 = *reinterpret_cast<const f32x4*>(ws + WS_ML + (size_t)(b * NH + rg) * NSPLIT * 2);
  const f32x4 ml1 = *reinterpret_cast<const f32x4*>(ws + WS_ML + (size_t)(b * NH + rg) * NSPLIT * 2 + 4);
  f32x4 fr[7];
#pragma unroll
  for (int j = 0; j < 7; ++j) {
    const int f = rg + 32 * j;
    const int fc = f < NFG ? f : NFG - 1;
    fr[j] = *reinterpret_cast<const f32x4*>(ws + WS_F + (size_t)(fc * NB + b) * DM + col);
  }
  u32 xg[5], gg[5];
#pragma unroll
  for (int j = 0; j < 5; ++j) {
    xg[j] = *reinterpret_cast<const u32*>(x + b * DM + 2 * (lane + 64 * j));
    gg[j] = *reinterpret_cast<const u32*>(gw + 2 * (lane + 64 * j));
  }
  float gs = 0.f;
#pragma unroll
  for (int j = 0; j < 5; ++j) gs += rbf(bflo(xg[j]) * bflo(gg[j])) + rbf(bfhi(xg[j]) * bfhi(gg[j]));
  gs = xrow_sum(row_sum(gs));
  gs = rbf(gs);
  const float g = rbf(1.f / (1.f + __expf(-gs)));
  const float M = fmaxf(fmaxf(ml0[0], ml0[2]), fmaxf(ml1[0], ml1[2]));
  const float e0 = __builtin_amdgcn_exp2f(ml0[0] - M), e1 = __builtin_amdgcn_exp2f(ml0[2] - M);
  const float e2 = __builtin_amdgcn_exp2f(ml1[0] - M), e3 = __builtin_amdgcn_exp2f(ml1[2] - M);
  const float L = e0 * ml0[1] + e1 * ml0[3] + e2 * ml1[1] + e3 * ml1[3];
  const float inv = 1.f / L;
  f32x4 a = (e0 * inv) * pr[0] + (e1 * inv) * pr[1] + (e2 * inv) * pr[2] + (e3 * inv) * pr[3];
  f32x4 f = fr[0] + fr[1] + fr[2] + fr[3] + fr[4] + fr[5];
  if (rg + 32 * 6 < NFG) f += fr[6];
#pragma unroll
  for (int k = 0; k < 4; ++k) {
    a[k] += dpp<XOR8>(a[k]); a[k] = xrow_sum(a[k]);
    f[k] += dpp<XOR8>(f[k]); f[k] = xrow_sum(f[k]);
  }
  if (lane < 8) {
    *reinterpret_cast<f32x4*>(&red[wave][0][c4 * 4]) = a;
    *reinterpret_cast<f32x4*>(&red[wave][1][c4 * 4]) = f;
  }
  __syncthreads();
  if (tid < 32) {
    const float av = rbf(red[0][0][tid] + red[1][0][tid] + red[2][0][tid] + red[3][0][tid]);
    const float fv = rbf(red[0][1][tid] + red[1][1][tid] + red[2][1][tid] + red[3][1][tid]);
    const float t1 = rbf(g * av), t2 = rbf(rbf(1.f - g) * fv);
    out[b * DM + cb * 32 + tid] = __builtin_bit_cast(u16, __float2bfloat16(t1 + t2));
  }
}

}

extern "C" void kernel_launch(void* const* d_in, const int* in_sizes, int n_in, void* d_out, int out_size, void* d_ws,
                              size_t ws_size, hipStream_t stream) {
  const u16* x = (const u16*)d_in[0];
  const u16* kc = (const u16*)d_in[1];
  const u16* vc = (const u16*)d_in[2];
  const u16* wq = (const u16*)d_in[3];
  const u16* wo = (const u16*)d_in[4];
  const u16* w1 = (const u16*)d_in[6];
  const u16* w2 = (const u16*)d_in[7];
  const u16* gw = (const u16*)d_in[8];
  float* ws = (float*)d_ws;
  k_main<<<dim3(NB * NH * NSPLIT), dim3(512), 0, stream>>>(x, kc, vc, wq, wo, w1, w2, ws, gw, (const u16*)d_out);
  k_fin<<<dim3(NB * 20), dim3(256), 0, stream>>>(x, gw, ws, (u16*)d_out);
}
```

```cpp
#include <hip/hip_runtime.h>
#include <hip/hip_bf16.h>
#include <stdint.h>

typedef unsigned int u32;
typedef unsigned short u16;
typedef __attribute__((ext_vector_type(4))) unsigned u32x4;
typedef __attribute__((ext_vector_type(2))) unsigned u32x2;
typedef __attribute__((ext_vector_type(4))) float f32x4;
typedef __attribute__((ext_vector_type(2))) float f32x2;
typedef __attribute__((ext_vector_type(2))) __bf16 bf16x2;

namespace {

constexpr int NB = 2, DM = 640, NH = 32, SK = 4096, DH = 128, FF = 1728;
constexpr int NSPLIT = 4, TOKWG = SK / NSPLIT, NWAVE = 8, TOKW = TOKWG / NWAVE;
constexpr int NFG = FF / 8;
constexpr float LOG2E = 1.4426950408889634f;
constexpr float SCALE = 0.088388347648318440f;

constexpr size_t WS_P = 0;
constexpr size_t WS_ML = WS_P + (size_t)NB * NH * NSPLIT * DM;
constexpr size_t WS_F = WS_ML + (size_t)NB * NH * NSPLIT * 2;

__device__ __forceinline__ float dot2(u32 a, u32 b, float acc) {
  return __builtin_amdgcn_fdot2_f32_bf16(__builtin_bit_cast(bf16x2, a), __builtin_bit_cast(bf16x2, b), acc, false);
}
__device__ __forceinline__ float bflo(u32 v) { return __builtin_bit_cast(float, v << 16); }
__device__ __forceinline__ float bfhi(u32 v) { return __builtin_bit_cast(float, v & 0xffff0000u); }
__device__ __forceinline__ float rbf(float x) { return __bfloat162float(__float2bfloat16(x)); }
__device__ __forceinline__ u32 pk_bf(float lo, float hi) {
  const u32 a = __builtin_bit_cast(u16, __float2bfloat16(lo));
  const u32 b = __builtin_bit_cast(u16, __float2bfloat16(hi));
  return a | (b << 16);
}
template <int CTRL> __device__ __forceinline__ float dpp(float x) {
  return __builtin_bit_cast(float, __builtin_amdgcn_mov_dpp(__builtin_bit_cast(int, x), CTRL, 0xf, 0xf, true));
}
constexpr int XOR1 = 0xB1, XOR2 = 0x4E, XOR7 = 0x141, XOR8 = 0x128, BCAST = 0x150;

__device__ __forceinline__ float xrow_sum(float x) {
  auto s = __builtin_amdgcn_permlane16_swap(__float_as_uint(x), __float_as_uint(x), false, false);
  x = __uint_as_float(s[0]) + __uint_as_float(s[1]);
  auto t = __builtin_amdgcn_permlane32_swap(__float_as_uint(x), __float_as_uint(x), false, false);
  return __uint_as_float(t[0]) + __uint_as_float(t[1]);
}
__device__ __forceinline__ float xrow_max(float x) {
  auto s = __builtin_amdgcn_permlane16_swap(__float_as_uint(x), __float_as_uint(x), false, false);
  x = fmaxf(__uint_as_float(s[0]), __uint_as_float(s[1]));
  auto t = __builtin_amdgcn_permlane32_swap(__float_as_uint(x), __float_as_uint(x), false, false);
  return fmaxf(__uint_as_float(t[0]), __uint_as_float(t[1]));
}
__device__ __forceinline__ float row_sum(float x) {
  x += dpp<XOR1>(x); x += dpp<XOR2>(x); x += dpp<XOR7>(x); x += dpp<XOR8>(x); return x;
}
__device__ __forceinline__ float row_max(float x) {
  x = fmaxf(x, dpp<XOR1>(x)); x = fmaxf(x, dpp<XOR2>(x)); x = fmaxf(x, dpp<XOR7>(x)); x = fmaxf(x, dpp<XOR8>(x)); return x;
}
__device__ __forceinline__ float tr_reduce16(float (&x)[16], int c) {
#define TR_STEP(HALF, CTRL, BIT)                                                     \
  _Pragma("unroll") for (int h = 0; h < (HALF); ++h) {                               \
    const float lo_ = x[h], hi_ = x[h + (HALF)];                                     \
    const bool up_ = (c & (BIT)) != 0;                                               \
    const float send_ = up_ ? lo_ : hi_, keep_ = up_ ? hi_ : lo_;                    \
    x[h] = keep_ + dpp<CTRL>(send_);                                                 \
  }
  TR_STEP(8, XOR8, 8) TR_STEP(4, XOR7, 4) TR_STEP(2, XOR2, 2) TR_STEP(1, XOR1, 1)
#undef TR_STEP
  return x[0];
}

#define SCHED_FENCE() __builtin_amdgcn_sched_barrier(0)
#define STAMP(i) do {} while (0)

#define KV_NT 1
__device__ __forceinline__ u32x4 ld_nt(const u16* ub, u32 off) {
  return __builtin_nontemporal_load(reinterpret_cast<const u32x4*>(reinterpret_cast<const char*>(ub) + off));
}
#define W_NT 0
#define KV_PAT 1
__device__ __forceinline__ u32x4 ld16(const u16* ub, u32 off) {
  return *reinterpret_cast<const u32x4*>(reinterpret_cast<const char*>(ub) + off);
}
__device__ __forceinline__ u32x2 ld8(const u16* ub, u32 off) {
  return *reinterpret_cast<const u32x2*>(reinterpret_cast<const char*>(ub) + off);
}
__device__ __forceinline__ u32 ld2(const u16* ub, u32 off) {
  return *reinterpret_cast<const u16*>(reinterpret_cast<const char*>(ub) + off);
}

constexpr int L_QP = 0;
constexpr int L_F1 = L_QP + NWAVE * DH;
constexpr int L_OP = L_F1 + NWAVE * 16;
constexpr int L_ML = L_OP + NWAVE * DH;
constexpr int L_RED = L_ML + NWAVE * 2;
constexpr int L_END = L_RED + NWAVE * DM;

__global__ void __launch_bounds__(512, 2)
k_main(const u16* __restrict__ x, const u16* __restrict__ kc, const u16* __restrict__ vc, const u16* __restrict__ wq,
       const u16* __restrict__ wo, const u16* __restrict__ w1, const u16* __restrict__ w2, float* __restrict__ ws) {
  __shared__ __attribute__((aligned(16))) float lds[L_END];
  const int bid = blockIdx.x, xcd = bid & 7, idx = bid >> 3;
  const int h = xcd * 4 + (idx >> 3), b = (idx >> 2) & 1, s = idx & 3;
  const int tid = threadIdx.x, lane = tid & 63, wave = __builtin_amdgcn_readfirstlane(tid >> 6);
  const int r = lane >> 4, c = lane & 15;
  const bool ffn_on = idx < 27;
  const int fg = xcd * 27 + (ffn_on ? idx : 26);

  STAMP(0);
  u32x2 xq[5];
#pragma unroll
  for (int j = 0; j < 5; ++j) xq[j] = ld8(x + b * DM + wave * 80 + 4 * j, r * 40);
  SCHED_FENCE();
  u32x4 Wq[20], KV[64], Wo[20];
#define KVP 1
  const int KVW = (wave >> 2) * 128 + (wave & 3);
#define KVO(i) ((i) * 4)
  const size_t kvbase = ((size_t)(b * NH + h) * SK + s * TOKWG + KVW * 4) * DH;
  const u16* kp = kc + kvbase;
  const u16* vp = vc + kvbase;
  const u32 loff = lane * 16;
#define LDKV(I) ld_nt(((I) < 32 ? kp + KVO(I) * 4 * DH : vp + KVO((I) - 32) * 4 * DH), loff)
  {
    const u32 off = (u32)(r * 20) * (NH * DH * 2) + 16 * c;
#pragma unroll
    for (int j = 0; j < 20; ++j) {
      Wq[j] = ld16(wq + (size_t)(wave * 80 + j) * (NH * DH) + h * DH, off);
      SCHED_FENCE();
      KV[j] = LDKV(j);
      SCHED_FENCE();
    }
  }
#pragma unroll
  for (int i = 20; i < 24; ++i) { KV[i] = LDKV(i); SCHED_FENCE(); }
  const u16* wop = wo + (size_t)(h * DH + wave * 16) * DM;
  const u32 wooff = (u32)r * (4 * DM * 2) + 16 * c;
#define PRE 5
#define ISSUE_T(T)                                                                                  \
  do {                                                                                              \
    if ((T) >= 0 && (T) < 20) {                                                                     \
      SCHED_FENCE();                                                                                \
      Wo[T] = ld16(wop + (2 * (((T) >> 1) / 5) + ((T) & 1)) * DM + (((T) >> 1) % 5) * 128, wooff);  \
      SCHED_FENCE();                                                                                \
      _Pragma("unroll") for (int t_ = 0; t_ < 2; ++t_) {                                            \
        const int kv_ = 24 + 2 * (T) + t_;                                                          \
        KV[kv_] = LDKV(kv_);                                                                        \
        SCHED_FENCE();                                                                              \
      }                                                                                             \
    }                                                                                               \
  } while (0)
#define PERM_Q(Q)                                                                                   \
  do {                                                                                              \
    _Pragma("unroll") for (int k_ = 0; k_ < 4; ++k_) {                                              \
      const u32 a_ = Wo[2 * (Q)][k_], b_ = Wo[2 * (Q) + 1][k_];                                     \
      Wo[2 * (Q)][k_] = __builtin_amdgcn_perm(b_, a_, 0x05040100u);                                 \
      Wo[2 * (Q) + 1][k_] = __builtin_amdgcn_perm(b_, a_, 0x07060302u);                             \
    }                                                                                               \
  } while (0)
#define AFTER_KV(I)                                                                                 \
  do {                                                                                              \
    if ((I) % 3 == 2) ISSUE_T(PRE + (I) / 3);                                                       \
    if ((I) >= 26 && ((I) - 26) % 4 == 0) PERM_Q(((I) - 26) / 4);                                   \
  } while (0)

#define ABAR 0
  STAMP(1);
  {
    float qa[8];
#pragma unroll
    for (int k = 0; k < 8; ++k) qa[k] = 0.f;
#pragma unroll
    for (int j = 0; j < 20; ++j) {
      const u32 xw = xq[j >> 2][(j >> 1) & 1];
      const float xv = (j & 1) ? bfhi(xw) : bflo(xw);
#pragma unroll
      for (int k = 0; k < 4; ++k) {
        qa[2 * k] = fmaf(xv, bflo(Wq[j][k]), qa[2 * k]);
        qa[2 * k + 1] = fmaf(xv, bfhi(Wq[j][k]), qa[2 * k + 1]);
      }
    }
#pragma unroll
    for (int k = 0; k < 8; ++k) qa[k] = xrow_sum(qa[k]);
    if (r == 0) {
      *reinterpret_cast<f32x4*>(&lds[L_QP + wave * DH + 8 * c]) = f32x4{qa[0], qa[1], qa[2], qa[3]};
      *reinterpret_cast<f32x4*>(&lds[L_QP + wave * DH + 8 * c + 4]) = f32x4{qa[4], qa[5], qa[6], qa[7]};
    }
  }
  SCHED_FENCE();
  STAMP(2);
  const u32x4 w1a = ld16(w1 + (size_t)(wave * 80) * FF + fg * 8, (u32)lane * (FF * 2));
  const u32x4 w1b = ld16(w1 + (size_t)(wave * 80 + 64) * FF + fg * 8, (u32)c * (FF * 2));
  const u32 xa0 = ld2(x + wave * 80, 2 * lane), xa1 = ld2(x + DM + wave * 80, 2 * lane);
  const u32 xb0 = ld2(x + wave * 80 + 64, 2 * c), xb1 = ld2(x + DM + wave * 80 + 64, 2 * c);
  SCHED_FENCE();
#pragma unroll
  for (int T = 0; T < PRE; ++T) ISSUE_T(T);
  __syncthreads();
  STAMP(3);
  u32 qpk[4];
  {
    f32x4 q0 = {0.f, 0.f, 0.f, 0.f}, q1 = {0.f, 0.f, 0.f, 0.f};
#pragma unroll
    for (int w = 0; w < NWAVE; ++w) {
      q0 += *reinterpret_cast<const f32x4*>(&lds[L_QP + w * DH + 8 * c]);
      q1 += *reinterpret_cast<const f32x4*>(&lds[L_QP + w * DH + 8 * c + 4]);
    }
    qpk[0] = pk_bf(q0[0], q0[1]); qpk[1] = pk_bf(q0[2], q0[3]);
    qpk[2] = pk_bf(q1[0], q1[1]); qpk[3] = pk_bf(q1[2], q1[3]);
  }

  float sc[2];
  u32x2 w2r[8];
#pragma unroll
  for (int g = 0; g < 2; ++g) {
    float xx[16];
#pragma unroll
    for (int i = 0; i < 16; ++i) {
      const u32x4 kk = KV[g * 16 + i];
      float d = dot2(kk[0], qpk[0], 0.f);
      d = dot2(kk[1], qpk[1], d);
      d = dot2(kk[2], qpk[2], d);
      xx[i] = dot2(kk[3], qpk[3], d);
      AFTER_KV(g * 16 + i);
    }
    STAMP(4 + g);
    const float t = tr_reduce16(xx, c);
    sc[g] = rbf(rbf(t) * SCALE);
    if (g == 0) {
      SCHED_FENCE();
      float fa[16];
      const float a0 = bflo(xa0), a1 = bflo(xa1);
      const float b0 = lane < 16 ? bflo(xb0) : 0.f, b1 = lane < 16 ? bflo(xb1) : 0.f;
#pragma unroll
      for (int k = 0; k < 4; ++k) {
        fa[2 * k] = fmaf(a0, bflo(w1a[k]), b0 * bflo(w1b[k]));
        fa[2 * k + 1] = fmaf(a0, bfhi(w1a[k]), b0 * bfhi(w1b[k]));
        fa[8 + 2 * k] = fmaf(a1, bflo(w1a[k]), b1 * bflo(w1b[k]));
        fa[8 + 2 * k + 1] = fmaf(a1, bfhi(w1a[k]), b1 * bfhi(w1b[k]));
      }
      float ft = tr_reduce16(fa, c);
      ft = xrow_sum(ft);
      if (r == 0) lds[L_F1 + wave * 16 + c] = ft;
      SCHED_FENCE();
      const u32 col = (tid < 160 ? tid : 0) * 8;
#pragma unroll
      for (int j = 0; j < 8; ++j) w2r[j] = ld8(w2 + (size_t)(fg * 8 + j) * DM, col);
      SCHED_FENCE();
    }
  }
  {
    const float mw = xrow_max(row_max(fmaxf(sc[0], sc[1])));
    if (lane == 0) lds[L_ML + wave] = mw;
  }
  __syncthreads();
  float m2;
  {
    const f32x4 t0 = *reinterpret_cast<const f32x4*>(&lds[L_ML]), t1 = *reinterpret_cast<const f32x4*>(&lds[L_ML + 4]);
    m2 = fmaxf(fmaxf(fmaxf(t0[0], t0[1]), fmaxf(t0[2], t0[3])), fmaxf(fmaxf(t1[0], t1[1]), fmaxf(t1[2], t1[3]))) * LOG2E;
  }
  float p[2];
  p[0] = __builtin_amdgcn_exp2f(fmaf(sc[0], LOG2E, -m2));
  p[1] = __builtin_amdgcn_exp2f(fmaf(sc[1], LOG2E, -m2));
  const float lsum = xrow_sum(row_sum(p[0] + p[1]));
  if (lane == 0) lds[L_ML + 8 + wave] = lsum;

  {
    float hval;
    float t = 0.f;
#pragma unroll
    for (int w = 0; w < NWAVE; ++w) t += lds[L_F1 + w * 16 + c];
    t = rbf(t);
    hval = rbf(t / (1.f + __expf(-t)));
    float f0[4] = {0.f, 0.f, 0.f, 0.f}, f1[4] = {0.f, 0.f, 0.f, 0.f};
#define FFN2_STEP(J)                                                                  \
    {                                                                                 \
      const float h0 = dpp<BCAST + (J)>(hval), h1 = dpp<BCAST + 8 + (J)>(hval);       \
      const float w0 = bflo(w2r[J][0]), w1_ = bfhi(w2r[J][0]), w2_ = bflo(w2r[J][1]), w3 = bfhi(w2r[J][1]); \
      f0[0] = fmaf(h0, w0, f0[0]); f0[1] = fmaf(h0, w1_, f0[1]); f0[2] = fmaf(h0, w2_, f0[2]); f0[3] = fmaf(h0, w3, f0[3]); \
      f1[0] = fmaf(h1, w0, f1[0]); f1[1] = fmaf(h1, w1_, f1[1]); f1[2] = fmaf(h1, w2_, f1[2]); f1[3] = fmaf(h1, w3, f1[3]); \
    }
    FFN2_STEP(0) FFN2_STEP(1) FFN2_STEP(2) FFN2_STEP(3) FFN2_STEP(4) FFN2_STEP(5) FFN2_STEP(6) FFN2_STEP(7)
#undef FFN2_STEP
    asm volatile("" : "+v"(f0[0]), "+v"(f0[1]), "+v"(f0[2]), "+v"(f0[3]), "+v"(f1[0]), "+v"(f1[1]), "+v"(f1[2]), "+v"(f1[3]));
    if (ffn_on && tid < 160) {
      float* fp = ws + WS_F + (size_t)fg * NB * DM + tid * 4;
      *reinterpret_cast<f32x4*>(fp) = f32x4{f0[0], f0[1], f0[2], f0[3]};
      *reinterpret_cast<f32x4*>(fp + DM) = f32x4{f1[0], f1[1], f1[2], f1[3]};
    }
  }
  SCHED_FENCE();

  float acc[8];
#pragma unroll
  for (int k = 0; k < 8; ++k) acc[k] = 0.f;
#define PV_STEP(I)                                                                    \
  {                                                                                   \
    const float pb = dpp<BCAST + ((I) & 15)>(p[(I) >> 4]);                            \
    const u32x4 vv = KV[32 + (I)];                                                    \
    _Pragma("unroll") for (int k = 0; k < 4; ++k) {                                   \
      acc[2 * k] = fmaf(pb, bflo(vv[k]), acc[2 * k]);                                 \
      acc[2 * k + 1] = fmaf(pb, bfhi(vv[k]), acc[2 * k + 1]);                         \
    }                                                                                 \
    AFTER_KV(32 + (I));                                                               \
  }
  PV_STEP(0) PV_STEP(1) PV_STEP(2) PV_STEP(3) PV_STEP(4) PV_STEP(5) PV_STEP(6) PV_STEP(7)
  PV_STEP(8) PV_STEP(9) PV_STEP(10) PV_STEP(11) PV_STEP(12) PV_STEP(13) PV_STEP(14) PV_STEP(15)
  PV_STEP(16) PV_STEP(17) PV_STEP(18) PV_STEP(19) PV_STEP(20) PV_STEP(21) PV_STEP(22) PV_STEP(23)
  PV_STEP(24) PV_STEP(25) PV_STEP(26) PV_STEP(27) PV_STEP(28) PV_STEP(29) PV_STEP(30) PV_STEP(31)
#undef PV_STEP
  STAMP(6);
#pragma unroll
  for (int k = 0; k < 8; ++k) acc[k] = xrow_sum(acc[k]);
  if (r == 0) {
    *reinterpret_cast<f32x4*>(&lds[L_OP + wave * DH + 8 * c]) = f32x4{acc[0], acc[1], acc[2], acc[3]};
    *reinterpret_cast<f32x4*>(&lds[L_OP + wave * DH + 8 * c + 4]) = f32x4{acc[4], acc[5], acc[6], acc[7]};
  }
  __syncthreads();

  STAMP(7);
  {
    f32x4 o4 = {0.f, 0.f, 0.f, 0.f};
#pragma unroll
    for (int w = 0; w < 8; ++w) o4 += *reinterpret_cast<const f32x4*>(&lds[L_OP + w * DH + (wave * 4 + r) * 4]);
    if (tid == 0) {
      const f32x4 t0 = *reinterpret_cast<const f32x4*>(&lds[L_ML + 8]), t1 = *reinterpret_cast<const f32x4*>(&lds[L_ML + 12]);
      const float L = ((t0[0] + t0[1]) + (t0[2] + t0[3])) + ((t1[0] + t1[1]) + (t1[2] + t1[3]));
      *reinterpret_cast<f32x2*>(ws + WS_ML + (size_t)((b * NH + h) * NSPLIT + s) * 2) = f32x2{m2, L};
    }
    const u32 o01 = pk_bf(o4[0], o4[1]), o23 = pk_bf(o4[2], o4[3]);
    float y[40];
#pragma unroll
    for (int seg = 0; seg < 5; ++seg)
#pragma unroll
      for (int k = 0; k < 4; ++k) {
        y[(2 * k) * 5 + seg] = dot2(Wo[2 * (5 + seg)][k], o23, dot2(Wo[2 * seg][k], o01, 0.f));
        y[(2 * k + 1) * 5 + seg] = dot2(Wo[2 * (5 + seg) + 1][k], o23, dot2(Wo[2 * seg + 1][k], o01, 0.f));
      }
    float z[20];
#pragma unroll
    for (int v = 0; v < 20; ++v) {
      auto t = __builtin_amdgcn_permlane32_swap(__float_as_uint(y[v]), __float_as_uint(y[v + 20]), false, false);
      z[v] = __uint_as_float(t[0]) + __uint_as_float(t[1]);
    }
    float u[10];
#pragma unroll
    for (int v = 0; v < 10; ++v) {
      auto t = __builtin_amdgcn_permlane16_swap(__float_as_uint(z[v]), __float_as_uint(z[v + 10]), false, false);
      u[v] = __uint_as_float(t[0]) + __uint_as_float(t[1]);
    }
#pragma unroll
    for (int seg = 0; seg < 5; ++seg)
      *reinterpret_cast<f32x2*>(&lds[L_RED + wave * DM + seg * 128 + 8 * c + 2 * r]) = f32x2{u[seg], u[5 + seg]};
  }
  __syncthreads();
  if (tid < 160) {
    f32x4 t = {0.f, 0.f, 0.f, 0.f};
#pragma unroll
    for (int w = 0; w < NWAVE; ++w) t += *reinterpret_cast<const f32x4*>(&lds[L_RED + w * DM + tid * 4]);
    *reinterpret_cast<f32x4*>(ws + WS_P + (size_t)((b * NH + h) * NSPLIT + s) * DM + tid * 4) = t;
  }
}

__global__ void __launch_bounds__(256)
k_fin(const u16* __restrict__ x, const u16* __restrict__ gw, const float* __restrict__ ws, u16* __restrict__ out) {
  __shared__ __attribute__((aligned(16))) float red[4][2][32];
  const int b = blockIdx.x / 20, cb = blockIdx.x % 20;
  const int tid = threadIdx.x, lane = tid & 63, wave = tid >> 6;
  const int c4 = tid & 7, rg = tid >> 3;
  const int col = cb * 32 + c4 * 4;
  f32x4 pr[4];
#pragma unroll
  for (int s = 0; s < 4; ++s)
    pr[s] = *reinterpret_cast<const f32x4*>(ws + WS_P + (size_t)((b * NH + rg) * NSPLIT + s) * DM + col);
  const f32x4 ml0 = *reinterpret_cast<const f32x4*>(ws + WS_ML + (size_t)(b * NH + rg) * NSPLIT * 2);
  const f32x4 ml1 = *reinterpret_cast<const f32x4*>(ws + WS_ML + (size_t)(b * NH + rg) * NSPLIT * 2 + 4);
  f32x4 fr[7];
#pragma unroll
  for (int j = 0; j < 7; ++j) {
    const int f = rg + 32 * j;
    const int fc = f < NFG ? f : NFG - 1;
    fr[j] = *reinterpret_cast<const f32x4*>(ws + WS_F + (size_t)(fc * NB + b) * DM + col);
  }
  u32 xg[5], gg[5];
#pragma unroll
  for (int j = 0; j < 5; ++j) {
    xg[j] = *reinterpret_cast<const u32*>(x + b * DM + 2 * (lane + 64 * j));
    gg[j] = *reinterpret_cast<const u32*>(gw + 2 * (lane + 64 * j));
  }
  float gs = 0.f;
#pragma unroll
  for (int j = 0; j < 5; ++j) gs += rbf(bflo(xg[j]) * bflo(gg[j])) + rbf(bfhi(xg[j]) * bfhi(gg[j]));
  gs = xrow_sum(row_sum(gs));
  gs = rbf(gs);
  const float g = rbf(1.f / (1.f + __expf(-gs)));
  const float M = fmaxf(fmaxf(ml0[0], ml0[2]), fmaxf(ml1[0], ml1[2]));
  const float e0 = __builtin_amdgcn_exp2f(ml0[0] - M), e1 = __builtin_amdgcn_exp2f(ml0[2] - M);
  const float e2 = __builtin_amdgcn_exp2f(ml1[0] - M), e3 = __builtin_amdgcn_exp2f(ml1[2] - M);
  const float L = e0 * ml0[1] + e1 * ml0[3] + e2 * ml1[1] + e3 * ml1[3];
  const float inv = 1.f / L;
  f32x4 a = (e0 * inv) * pr[0] + (e1 * inv) * pr[1] + (e2 * inv) * pr[2] + (e3 * inv) * pr[3];
  f32x4 f = fr[0] + fr[1] + fr[2] + fr[3] + fr[4] + fr[5];
  if (rg + 32 * 6 < NFG) f += fr[6];
#pragma unroll
  for (int k = 0; k < 4; ++k) {
    a[k] += dpp<XOR8>(a[k]); a[k] = xrow_sum(a[k]);
    f[k] += dpp<XOR8>(f[k]); f[k] = xrow_sum(f[k]);
  }
  if (lane < 8) {
    *reinterpret_cast<f32x4*>(&red[wave][0][c4 * 4]) = a;
    *reinterpret_cast<f32x4*>(&red[wave][1][c4 * 4]) = f;
  }
  __syncthreads();
  if (tid < 32) {
    const float av = rbf(red[0][0][tid] + red[1][0][tid] + red[2][0][tid] + red[3][0][tid]);
    const float fv = rbf(red[0][1][tid] + red[1][1][tid] + red[2][1][tid] + red[3][1][tid]);
    const float t1 = rbf(g * av), t2 = rbf(rbf(1.f - g) * fv);
    out[b * DM + cb * 32 + tid] = __builtin_bit_cast(u16, __float2bfloat16(t1 + t2));
  }
}

}

extern "C" void kernel_launch(void* const* d_in, const int* in_sizes, int n_in, void* d_out, int out_size, void* d_ws,
                              size_t ws_size, hipStream_t stream) {
  const u16* x = (const u16*)d_in[0];
  const u16* kc = (const u16*)d_in[1];
  const u16* vc = (const u16*)d_in[2];
  const u16* wq = (const u16*)d_in[3];
  const u16* wo = (const u16*)d_in[4];
  const u16* w1 = (const u16*)d_in[6];
  const u16* w2 = (const u16*)d_in[7];
  const u16* gw = (const u16*)d_in[8];
  float* ws = (float*)d_ws;
  k_main<<<dim3(NB * NH * NSPLIT), dim3(512), 0, stream>>>(x, kc, vc, wq, wo, w1, w2, ws);
  k_fin<<<dim3(NB * 20), dim3(256), 0, stream>>>(x, gw, ws, (u16*)d_out);
}
```

```cpp
#include <hip/hip_runtime.h>
#include <hip/hip_bf16.h>
#include <stdint.h>

typedef unsigned int u32;
typedef unsigned short u16;
typedef __attribute__((ext_vector_type(4))) unsigned u32x4;
typedef __attribute__((ext_vector_type(2))) unsigned u32x2;
typedef __attribute__((ext_vector_type(4))) float f32x4;
typedef __attribute__((ext_vector_type(2))) float f32x2;
typedef __attribute__((ext_vector_type(2))) __bf16 bf16x2;

namespace {

constexpr int NB = 2, DM = 640, NH = 32, SK = 4096, DH = 128, FF = 1728;
constexpr int NSPLIT = 4, TOKWG = SK / NSPLIT, NWAVE = 8, TOKW = TOKWG / NWAVE;
constexpr int NFG = FF / 8;
constexpr float LOG2E = 1.4426950408889634f;
constexpr float SCALE = 0.088388347648318440f;

constexpr size_t WS_P = 0;
constexpr size_t WS_ML = WS_P + (size_t)NB * NH * NSPLIT * DM;
constexpr size_t WS_F = WS_ML + (size_t)NB * NH * NSPLIT * 2;

__device__ __forceinline__ float dot2(u32 a, u32 b, float acc) {
  return __builtin_amdgcn_fdot2_f32_bf16(__builtin_bit_cast(bf16x2, a), __builtin_bit_cast(bf16x2, b), acc, false);
}
__device__ __forceinline__ float bflo(u32 v) { return __builtin_bit_cast(float, v << 16); }
__device__ __forceinline__ float bfhi(u32 v) { return __builtin_bit_cast(float, v & 0xffff0000u); }
__device__ __forceinline__ float rbf(float x) { return __bfloat162float(__float2bfloat16(x)); }
__device__ __forceinline__ u32 pk_bf(float lo, float hi) {
  const u32 a = __builtin_bit_cast(u16, __float2bfloat16(lo));
  const u32 b = __builtin_bit_cast(u16, __float2bfloat16(hi));
  return a | (b << 16);
}
template <int CTRL> __device__ __forceinline__ float dpp(float x) {
  return __builtin_bit_cast(float, __builtin_amdgcn_mov_dpp(__builtin_bit_cast(int, x), CTRL, 0xf, 0xf, true));
}
constexpr int XOR1 = 0xB1, XOR2 = 0x4E, XOR7 = 0x141, XOR8 = 0x128, BCAST = 0x150;

__device__ __forceinline__ float xrow_sum(float x) {
  auto s = __builtin_amdgcn_permlane16_swap(__float_as_uint(x), __float_as_uint(x), false, false);
  x = __uint_as_float(s[0]) + __uint_as_float(s[1]);
  auto t = __builtin_amdgcn_permlane32_swap(__float_as_uint(x), __float_as_uint(x), false, false);
  return __uint_as_float(t[0]) + __uint_as_float(t[1]);
}
__device__ __forceinline__ float xrow_max(float x) {
  auto s = __builtin_amdgcn_permlane16_swap(__float_as_uint(x), __float_as_uint(x), false, false);
  x = fmaxf(__uint_as_float(s[0]), __uint_as_float(s[1]));
  auto t = __builtin_amdgcn_permlane32_swap(__float_as_uint(x), __float_as_uint(x), false, false);
  return fmaxf(__uint_as_float(t[0]), __uint_as_float(t[1]));
}
__device__ __forceinline__ float row_sum(float x) {
  x += dpp<XOR1>(x); x += dpp<XOR2>(x); x += dpp<XOR7>(x); x += dpp<XOR8>(x); return x;
}
__device__ __forceinline__ float row_max(float x) {
  x = fmaxf(x, dpp<XOR1>(x)); x = fmaxf(x, dpp<XOR2>(x)); x = fmaxf(x, dpp<XOR7>(x)); x = fmaxf(x, dpp<XOR8>(x)); return x;
}
__device__ __forceinline__ float tr_reduce16(float (&x)[16], int c) {
#define TR_STEP(HALF, CTRL, BIT)                                                     \
  _Pragma("unroll") for (int h = 0; h < (HALF); ++h) {                               \
    const float lo_ = x[h], hi_ = x[h + (HALF)];                                     \
    const bool up_ = (c & (BIT)) != 0;                                               \
    const float send_ = up_ ? lo_ : hi_, keep_ = up_ ? hi_ : lo_;                    \
    x[h] = keep_ + dpp<CTRL>(send_);                                                 \
  }
  TR_STEP(8, XOR8, 8) TR_STEP(4, XOR7, 4) TR_STEP(2, XOR2, 2) TR_STEP(1, XOR1, 1)
#undef TR_STEP
  return x[0];
}

#define SCHED_FENCE() __builtin_amdgcn_sched_barrier(0)
#define STAMP(i) do {} while (0)

#define KV_NT 1
__device__ __forceinline__ u32x4 ld_nt(const u16* ub, u32 off) {
  return __builtin_nontemporal_load(reinterpret_cast<const u32x4*>(reinterpret_cast<const char*>(ub) + off));
}
#define W_NT 0
#define KV_PAT 1
__device__ __forceinline__ u32x4 ld16(const u16* ub, u32 off) {
  return *reinterpret_cast<const u32x4*>(reinterpret_cast<const char*>(ub) + off);
}
__device__ __forceinline__ u32x2 ld8(const u16* ub, u32 off) {
  return *reinterpret_cast<const u32x2*>(reinterpret_cast<const char*>(ub) + off);
}
__device__ __forceinline__ u32 ld2(const u16* ub, u32 off) {
  return *reinterpret_cast<const u16*>(reinterpret_cast<const char*>(ub) + off);
}

constexpr int L_QP = 0;
constexpr int L_F1 = L_QP + NWAVE * DH;
constexpr int L_OP = L_F1 + NWAVE * 16;
constexpr int L_ML = L_OP + NWAVE * DH;
constexpr int L_RED = L_ML + NWAVE * 2;
constexpr int L_END = L_RED + NWAVE * DM;

__global__ void __launch_bounds__(512, 2)
k_main(const u16* __restrict__ x, const u16* __restrict__ kc, const u16* __restrict__ vc, const u16* __restrict__ wq,
       const u16* __restrict__ wo, const u16* __restrict__ w1, const u16* __restrict__ w2, float* __restrict__ ws) {
  __shared__ __attribute__((aligned(16))) float lds[L_END];
  const int bid = blockIdx.x, xcd = bid & 7, idx = bid >> 3;
  const int h = xcd * 4 + (idx >> 3), b = (idx >> 2) & 1, s = idx & 3;
  const int tid = threadIdx.x, lane = tid & 63, wave = __builtin_amdgcn_readfirstlane(tid >> 6);
  const int r = lane >> 4, c = lane & 15;
  const bool ffn_on = idx < 27;
  const int fg = xcd * 27 + (ffn_on ? idx : 26);

  STAMP(0);
  u32x2 xq[5];
#pragma unroll
  for (int j = 0; j < 5; ++j) xq[j] = ld8(x + b * DM + wave * 80 + 4 * j, r * 40);
  SCHED_FENCE();
  u32x4 Wq[20], KV[64], Wo[20];
#define KVP 1
  const int KVW = (wave >> 2) * 128 + (wave & 3);
#define KVO(i) ((i) * 4)
  const size_t kvbase = ((size_t)(b * NH + h) * SK + s * TOKWG + KVW * 4) * DH;
  const u16* kp = kc + kvbase;
  const u16* vp = vc + kvbase;
  const u32 loff = lane * 16;
#define KNT 1
#define VNT 1
#define LDKV(I) ((I) < 32 ? (KNT ? ld_nt(kp + KVO(I) * 4 * DH, loff) : ld16(kp + KVO(I) * 4 * DH, loff)) \
                          : (VNT ? ld_nt(vp + KVO((I) - 32) * 4 * DH, loff) : ld16(vp + KVO((I) - 32) * 4 * DH, loff)))
  {
    const u32 off = (u32)(r * 20) * (NH * DH * 2) + 16 * c;
#pragma unroll
    for (int j = 0; j < 20; ++j) {
      Wq[j] = ld16(wq + (size_t)(wave * 80 + j) * (NH * DH) + h * DH, off);
      SCHED_FENCE();
      KV[j] = LDKV(j);
      SCHED_FENCE();
    }
  }
#pragma unroll
  for (int i = 20; i < 24; ++i) { KV[i] = LDKV(i); SCHED_FENCE(); }
  const u16* wop = wo + (size_t)(h * DH + wave * 16) * DM;
  const u32 wooff = (u32)r * (4 * DM * 2) + 16 * c;
#define PRE 5
#define ISSUE_T(T)                                                                                  \
  do {                                                                                              \
    if ((T) >= 0 && (T) < 20) {                                                                     \
      SCHED_FENCE();                                                                                \
      Wo[T] = ld16(wop + (2 * (((T) >> 1) / 5) + ((T) & 1)) * DM + (((T) >> 1) % 5) * 128, wooff);  \
      SCHED_FENCE();                                                                                \
      _Pragma("unroll") for (int t_ = 0; t_ < 2; ++t_) {                                            \
        const int kv_ = 24 + 2 * (T) + t_;                                                          \
        KV[kv_] = LDKV(kv_);                                                                        \
        SCHED_FENCE();                                                                              \
      }                                                                                             \
    }                                                                                               \
  } while (0)
#define PERM_Q(Q)                                                                                   \
  do {                                                                                              \
    _Pragma("unroll") for (int k_ = 0; k_ < 4; ++k_) {                                              \
      const u32 a_ = Wo[2 * (Q)][k_], b_ = Wo[2 * (Q) + 1][k_];                                     \
      Wo[2 * (Q)][k_] = __builtin_amdgcn_perm(b_, a_, 0x05040100u);                                 \
      Wo[2 * (Q) + 1][k_] = __builtin_amdgcn_perm(b_, a_, 0x07060302u);                             \
    }                                                                                               \
    asm volatile("" : "+v"(Wo[2 * (Q)]), "+v"(Wo[2 * (Q) + 1]));                                     \
  } while (0)
#define AFTER_KV(I)                                                                                 \
  do {                                                                                              \
    if ((I) % 3 == 2) ISSUE_T(PRE + (I) / 3);                                                       \
    if ((I) >= 26 && ((I) - 26) % 4 == 0) PERM_Q(((I) - 26) / 4);                                   \
  } while (0)

#define ABAR 0
  STAMP(1);
  {
    float qa[8];
#pragma unroll
    for (int k = 0; k < 8; ++k) qa[k] = 0.f;
#pragma unroll
    for (int j = 0; j < 20; ++j) {
      const u32 xw = xq[j >> 2][(j >> 1) & 1];
      const float xv = (j & 1) ? bfhi(xw) : bflo(xw);
#pragma unroll
      for (int k = 0; k < 4; ++k) {
        qa[2 * k] = fmaf(xv, bflo(Wq[j][k]), qa[2 * k]);
        qa[2 * k + 1] = fmaf(xv, bfhi(Wq[j][k]), qa[2 * k + 1]);
      }
    }
#pragma unroll
    for (int k = 0; k < 8; ++k) qa[k] = xrow_sum(qa[k]);
    if (r == 0) {
      *reinterpret_cast<f32x4*>(&lds[L_QP + wave * DH + 8 * c]) = f32x4{qa[0], qa[1], qa[2], qa[3]};
      *reinterpret_cast<f32x4*>(&lds[L_QP + wave * DH + 8 * c + 4]) = f32x4{qa[4], qa[5], qa[6], qa[7]};
    }
  }
  SCHED_FENCE();
  STAMP(2);
  const u32x4 w1a = ld16(w1 + (size_t)(wave * 80) * FF + fg * 8, (u32)lane * (FF * 2));
  const u32x4 w1b = ld16(w1 + (size_t)(wave * 80 + 64) * FF + fg * 8, (u32)c * (FF * 2));
  const u32 xa0 = ld2(x + wave * 80, 2 * lane), xa1 = ld2(x + DM + wave * 80, 2 * lane);
  const u32 xb0 = ld2(x + wave * 80 + 64, 2 * c), xb1 = ld2(x + DM + wave * 80 + 64, 2 * c);
  SCHED_FENCE();
#pragma unroll
  for (int T = 0; T < PRE; ++T) ISSUE_T(T);
  __syncthreads();
  STAMP(3);
  u32 qpk[4];
  {
    f32x4 q0 = {0.f, 0.f, 0.f, 0.f}, q1 = {0.f, 0.f, 0.f, 0.f};
#pragma unroll
    for (int w = 0; w < NWAVE; ++w) {
      q0 += *reinterpret_cast<const f32x4*>(&lds[L_QP + w * DH + 8 * c]);
      q1 += *reinterpret_cast<const f32x4*>(&lds[L_QP + w * DH + 8 * c + 4]);
    }
    qpk[0] = pk_bf(q0[0], q0[1]); qpk[1] = pk_bf(q0[2], q0[3]);
    qpk[2] = pk_bf(q1[0], q1[1]); qpk[3] = pk_bf(q1[2], q1[3]);
  }

  float sc[2];
  u32x2 w2r[8];
#pragma unroll
  for (int g = 0; g < 2; ++g) {
    float xx[16];
#pragma unroll
    for (int i = 0; i < 16; ++i) {
      const u32x4 kk = KV[g * 16 + i];
      float d = dot2(kk[0], qpk[0], 0.f);
      d = dot2(kk[1], qpk[1], d);
      d = dot2(kk[2], qpk[2], d);
      xx[i] = dot2(kk[3], qpk[3], d);
      AFTER_KV(g * 16 + i);
    }
    STAMP(4 + g);
    const float t = tr_reduce16(xx, c);
    sc[g] = rbf(rbf(t) * SCALE);
    if (g == 0) {
      SCHED_FENCE();
      float fa[16];
      const float a0 = bflo(xa0), a1 = bflo(xa1);
      const float b0 = lane < 16 ? bflo(xb0) : 0.f, b1 = lane < 16 ? bflo(xb1) : 0.f;
#pragma unroll
      for (int k = 0; k < 4; ++k) {
        fa[2 * k] = fmaf(a0, bflo(w1a[k]), b0 * bflo(w1b[k]));
        fa[2 * k + 1] = fmaf(a0, bfhi(w1a[k]), b0 * bfhi(w1b[k]));
        fa[8 + 2 * k] = fmaf(a1, bflo(w1a[k]), b1 * bflo(w1b[k]));
        fa[8 + 2 * k + 1] = fmaf(a1, bfhi(w1a[k]), b1 * bfhi(w1b[k]));
      }
      float ft = tr_reduce16(fa, c);
      ft = xrow_sum(ft);
      if (r == 0) lds[L_F1 + wave * 16 + c] = ft;
      SCHED_FENCE();
      const u32 col = (tid < 160 ? tid : 0) * 8;
#pragma unroll
      for (int j = 0; j < 8; ++j) w2r[j] = ld8(w2 + (size_t)(fg * 8 + j) * DM, col);
      SCHED_FENCE();
    }
  }
  {
    const float mw = xrow_max(row_max(fmaxf(sc[0], sc[1])));
    if (lane == 0) lds[L_ML + wave] = mw;
  }
  __syncthreads();
  float m2;
  {
    const f32x4 t0 = *reinterpret_cast<const f32x4*>(&lds[L_ML]), t1 = *reinterpret_cast<const f32x4*>(&lds[L_ML + 4]);
    m2 = fmaxf(fmaxf(fmaxf(t0[0], t0[1]), fmaxf(t0[2], t0[3])), fmaxf(fmaxf(t1[0], t1[1]), fmaxf(t1[2], t1[3]))) * LOG2E;
  }
  float p[2];
  p[0] = __builtin_amdgcn_exp2f(fmaf(sc[0], LOG2E, -m2));
  p[1] = __builtin_amdgcn_exp2f(fmaf(sc[1], LOG2E, -m2));
  const float lsum = xrow_sum(row_sum(p[0] + p[1]));
  if (lane == 0) lds[L_ML + 8 + wave] = lsum;

  {
    float hval;
    float t = 0.f;
#pragma unroll
    for (int w = 0; w < NWAVE; ++w) t += lds[L_F1 + w * 16 + c];
    t = rbf(t);
    hval = rbf(t / (1.f + __expf(-t)));
    float f0[4] = {0.f, 0.f, 0.f, 0.f}, f1[4] = {0.f, 0.f, 0.f, 0.f};
#define FFN2_STEP(J)                                                                  \
    {                                                                                 \
      const float h0 = dpp<BCAST + (J)>(hval), h1 = dpp<BCAST + 8 + (J)>(hval);       \
      const float w0 = bflo(w2r[J][0]), w1_ = bfhi(w2r[J][0]), w2_ = bflo(w2r[J][1]), w3 = bfhi(w2r[J][1]); \
      f0[0] = fmaf(h0, w0, f0[0]); f0[1] = fmaf(h0, w1_, f0[1]); f0[2] = fmaf(h0, w2_, f0[2]); f0[3] = fmaf(h0, w3, f0[3]); \
      f1[0] = fmaf(h1, w0, f1[0]); f1[1] = fmaf(h1, w1_, f1[1]); f1[2] = fmaf(h1, w2_, f1[2]); f1[3] = fmaf(h1, w3, f1[3]); \
    }
    FFN2_STEP(0) FFN2_STEP(1) FFN2_STEP(2) FFN2_STEP(3) FFN2_STEP(4) FFN2_STEP(5) FFN2_STEP(6) FFN2_STEP(7)
#undef FFN2_STEP
    asm volatile("" : "+v"(f0[0]), "+v"(f0[1]), "+v"(f0[2]), "+v"(f0[3]), "+v"(f1[0]), "+v"(f1[1]), "+v"(f1[2]), "+v"(f1[3]));
    if (ffn_on && tid < 160) {
      float* fp = ws + WS_F + (size_t)fg * NB * DM + tid * 4;
      *reinterpret_cast<f32x4*>(fp) = f32x4{f0[0], f0[1], f0[2], f0[3]};
      *reinterpret_cast<f32x4*>(fp + DM) = f32x4{f1[0], f1[1], f1[2], f1[3]};
    }
  }
  SCHED_FENCE();

  float acc[8];
#pragma unroll
  for (int k = 0; k < 8; ++k) acc[k] = 0.f;
#define PV_STEP(I)                                                                    \
  {                                                                                   \
    const float pb = dpp<BCAST + ((I) & 15)>(p[(I) >> 4]);                            \
    const u32x4 vv = KV[32 + (I)];                                                    \
    _Pragma("unroll") for (int k = 0; k < 4; ++k) {                                   \
      acc[2 * k] = fmaf(pb, bflo(vv[k]), acc[2 * k]);                                 \
      acc[2 * k + 1] = fmaf(pb, bfhi(vv[k]), acc[2 * k + 1]);                         \
    }                                                                                 \
    AFTER_KV(32 + (I));                                                               \
  }
  PV_STEP(0) PV_STEP(1) PV_STEP(2) PV_STEP(3) PV_STEP(4) PV_STEP(5) PV_STEP(6) PV_STEP(7)
  PV_STEP(8) PV_STEP(9) PV_STEP(10) PV_STEP(11) PV_STEP(12) PV_STEP(13) PV_STEP(14) PV_STEP(15)
  PV_STEP(16) PV_STEP(17) PV_STEP(18) PV_STEP(19) PV_STEP(20) PV_STEP(21) PV_STEP(22) PV_STEP(23)
  PV_STEP(24) PV_STEP(25) PV_STEP(26) PV_STEP(27)
  float y[40];
#pragma unroll
  for (int v = 0; v < 40; v += 8) {
#pragma unroll
    for (int k = 0; k < 8; ++k) y[v + k] = 0.f;
    asm volatile("" : "+v"(y[v]), "+v"(y[v + 1]), "+v"(y[v + 2]), "+v"(y[v + 3]), "+v"(y[v + 4]), "+v"(y[v + 5]), "+v"(y[v + 6]), "+v"(y[v + 7]));
  }
  SCHED_FENCE();
  PV_STEP(28) PV_STEP(29) PV_STEP(30) PV_STEP(31)
#undef PV_STEP
  STAMP(6);
  {
    float z4[4], z2[2];
#pragma unroll
    for (int k = 0; k < 4; ++k) {
      auto t = __builtin_amdgcn_permlane32_swap(__float_as_uint(acc[k]), __float_as_uint(acc[k + 4]), false, false);
      z4[k] = __uint_as_float(t[0]) + __uint_as_float(t[1]);
    }
#pragma unroll
    for (int k = 0; k < 2; ++k) {
      auto t = __builtin_amdgcn_permlane16_swap(__float_as_uint(z4[k]), __float_as_uint(z4[k + 2]), false, false);
      z2[k] = __uint_as_float(t[0]) + __uint_as_float(t[1]);
    }
    *reinterpret_cast<f32x2*>(&lds[L_OP + wave * DH + 8 * c + 2 * r]) = f32x2{z2[0], z2[1]};
  }
  __syncthreads();

  STAMP(7);
  {
    f32x4 o4 = {0.f, 0.f, 0.f, 0.f};
#pragma unroll
    for (int w = 0; w < 8; ++w) o4 += *reinterpret_cast<const f32x4*>(&lds[L_OP + w * DH + (wave * 4 + r) * 4]);
    const u32 o01 = pk_bf(o4[0], o4[1]), o23 = pk_bf(o4[2], o4[3]);
#pragma unroll
    for (int seg = 0; seg < 5; ++seg)
#pragma unroll
      for (int k = 0; k < 4; ++k) {
        y[(2 * k) * 5 + seg] = dot2(Wo[2 * (5 + seg)][k], o23, dot2(Wo[2 * seg][k], o01, y[(2 * k) * 5 + seg]));
        y[(2 * k + 1) * 5 + seg] = dot2(Wo[2 * (5 + seg) + 1][k], o23, dot2(Wo[2 * seg + 1][k], o01, y[(2 * k + 1) * 5 + seg]));
      }
    float z[20];
#pragma unroll
    for (int v = 0; v < 20; ++v) {
      auto t = __builtin_amdgcn_permlane32_swap(__float_as_uint(y[v]), __float_as_uint(y[v + 20]), false, false);
      z[v] = __uint_as_float(t[0]) + __uint_as_float(t[1]);
    }
    float u[10];
#pragma unroll
    for (int v = 0; v < 10; ++v) {
      auto t = __builtin_amdgcn_permlane16_swap(__float_as_uint(z[v]), __float_as_uint(z[v + 10]), false, false);
      u[v] = __uint_as_float(t[0]) + __uint_as_float(t[1]);
    }
#pragma unroll
    for (int seg = 0; seg < 5; ++seg)
      *reinterpret_cast<f32x2*>(&lds[L_RED + wave * DM + seg * 128 + 8 * c + 2 * r]) = f32x2{u[seg], u[5 + seg]};
  }
  __syncthreads();
  if (tid < 160) {
    f32x4 t = {0.f, 0.f, 0.f, 0.f};
#pragma unroll
    for (int w = 0; w < NWAVE; ++w) t += *reinterpret_cast<const f32x4*>(&lds[L_RED + w * DM + tid * 4]);
    *reinterpret_cast<f32x4*>(ws + WS_P + (size_t)((b * NH + h) * NSPLIT + s) * DM + tid * 4) = t;
  }
  if (tid == 256) {
      const f32x4 t0 = *reinterpret_cast<const f32x4*>(&lds[L_ML + 8]), t1 = *reinterpret_cast<const f32x4*>(&lds[L_ML + 12]);
      const float L = ((t0[0] + t0[1]) + (t0[2] + t0[3])) + ((t1[0] + t1[1]) + (t1[2] + t1[3]));
      *reinterpret_cast<f32x2*>(ws + WS_ML + (size_t)((b * NH + h) * NSPLIT + s) * 2) = f32x2{m2, L};
    }
}

__global__ void __launch_bounds__(256)
k_fin(const u16* __restrict__ x, const u16* __restrict__ gw, const float* __restrict__ ws, u16* __restrict__ out) {
  __shared__ __attribute__((aligned(16))) float red[4][2][32];
  const int b = blockIdx.x / 20, cb = blockIdx.x % 20;
  const int tid = threadIdx.x, lane = tid & 63, wave = tid >> 6;
  const int c4 = tid & 7, rg = tid >> 3;
  const int col = cb * 32 + c4 * 4;
  f32x4 pr[4];
#pragma unroll
  for (int s = 0; s < 4; ++s)
    pr[s] = *reinterpret_cast<const f32x4*>(ws + WS_P + (size_t)((b * NH + rg) * NSPLIT + s) * DM + col);
  const f32x4 ml0 = *reinterpret_cast<const f32x4*>(ws + WS_ML + (size_t)(b * NH + rg) * NSPLIT * 2);
  const f32x4 ml1 = *reinterpret_cast<const f32x4*>(ws + WS_ML + (size_t)(b * NH + rg) * NSPLIT * 2 + 4);
  f32x4 fr[7];
#pragma unroll
  for (int j = 0; j < 7; ++j) {
    const int f = rg + 32 * j;
    const int fc = f < NFG ? f : NFG - 1;
    fr[j] = *reinterpret_cast<const f32x4*>(ws + WS_F + (size_t)(fc * NB + b) * DM + col);
  }
  u32 xg[5], gg[5];
#pragma unroll
  for (int j = 0; j < 5; ++j) {
    xg[j] = *reinterpret_cast<const u32*>(x + b * DM + 2 * (lane + 64 * j));
    gg[j] = *reinterpret_cast<const u32*>(gw + 2 * (lane + 64 * j));
  }
  float gs = 0.f;
#pragma unroll
  for (int j = 0; j < 5; ++j) gs += rbf(bflo(xg[j]) * bflo(gg[j])) + rbf(bfhi(xg[j]) * bfhi(gg[j]));
  gs = xrow_sum(row_sum(gs));
  gs = rbf(gs);
  const float g = rbf(1.f / (1.f + __expf(-gs)));
  const float M = fmaxf(fmaxf(ml0[0], ml0[2]), fmaxf(ml1[0], ml1[2]));
  const float e0 = __builtin_amdgcn_exp2f(ml0[0] - M), e1 = __builtin_amdgcn_exp2f(ml0[2] - M);
  const float e2 = __builtin_amdgcn_exp2f(ml1[0] - M), e3 = __builtin_amdgcn_exp2f(ml1[2] - M);
  const float L = e0 * ml0[1] + e1 * ml0[3] + e2 * ml1[1] + e3 * ml1[3];
  const float inv = 1.f / L;
  f32x4 a = (e0 * inv) * pr[0] + (e1 * inv) * pr[1] + (e2 * inv) * pr[2] + (e3 * inv) * pr[3];
  f32x4 f = fr[0] + fr[1] + fr[2] + fr[3] + fr[4] + fr[5];
  if (rg + 32 * 6 < NFG) f += fr[6];
#pragma unroll
  for (int k = 0; k < 4; ++k) {
    a[k] += dpp<XOR8>(a[k]); a[k] = xrow_sum(a[k]);
    f[k] += dpp<XOR8>(f[k]); f[k] = xrow_sum(f[k]);
  }
  if (lane < 8) {
    *reinterpret_cast<f32x4*>(&red[wave][0][c4 * 4]) = a;
    *reinterpret_cast<f32x4*>(&red[wave][1][c4 * 4]) = f;
  }
  __syncthreads();
  if (tid < 32) {
    const float av = rbf(red[0][0][tid] + red[1][0][tid] + red[2][0][tid] + red[3][0][tid]);
    const float fv = rbf(red[0][1][tid] + red[1][1][tid] + red[2][1][tid] + red[3][1][tid]);
    const float t1 = rbf(g * av), t2 = rbf(rbf(1.f - g) * fv);
    out[b * DM + cb * 32 + tid] = __builtin_bit_cast(u16, __float2bfloat16(t1 + t2));
  }
}

}

extern "C" void kernel_launch(void* const* d_in, const int* in_sizes, int n_in, void* d_out, int out_size, void* d_ws,
                              size_t ws_size, hipStream_t stream) {
  const u16* x = (const u16*)d_in[0];
  const u16* kc = (const u16*)d_in[1];
  const u16* vc = (const u16*)d_in[2];
  const u16* wq = (const u16*)d_in[3];
  const u16* wo = (const u16*)d_in[4];
  const u16* w1 = (const u16*)d_in[6];
  const u16* w2 = (const u16*)d_in[7];
  const u16* gw = (const u16*)d_in[8];
  float* ws = (float*)d_ws;
  k_main<<<dim3(NB * NH * NSPLIT), dim3(512), 0, stream>>>(x, kc, vc, wq, wo, w1, w2, ws);
  k_fin<<<dim3(NB * 20), dim3(256), 0, stream>>>(x, gw, ws, (u16*)d_out);
}
```

```cpp
#include <hip/hip_runtime.h>
#include <hip/hip_bf16.h>
#include <stdint.h>

typedef unsigned int u32;
typedef unsigned short u16;
typedef __attribute__((ext_vector_type(4))) unsigned u32x4;
typedef __attribute__((ext_vector_type(2))) unsigned u32x2;
typedef __attribute__((ext_vector_type(4))) float f32x4;
typedef __attribute__((ext_vector_type(2))) float f32x2;
typedef __attribute__((ext_vector_type(2))) __bf16 bf16x2;

namespace {

constexpr int NB = 2, DM = 640, NH = 32, SK = 4096, DH = 128, FF = 1728;
constexpr int NSPLIT = 4, TOKWG = SK / NSPLIT, NWAVE = 8, TOKW = TOKWG / NWAVE;
constexpr int NFG = FF / 8;
constexpr float LOG2E = 1.4426950408889634f;
constexpr float SCALE = 0.088388347648318440f;

constexpr size_t WS_P = 0;
constexpr size_t WS_ML = WS_P + (size_t)NB * NH * NSPLIT * DM;
constexpr size_t WS_F = WS_ML + (size_t)NB * NH * NSPLIT * 2;

__device__ __forceinline__ float dot2(u32 a, u32 b, float acc) {
  return __builtin_amdgcn_fdot2_f32_bf16(__builtin_bit_cast(bf16x2, a), __builtin_bit_cast(bf16x2, b), acc, false);
}
__device__ __forceinline__ float bflo(u32 v) { return __builtin_bit_cast(float, v << 16); }
__device__ __forceinline__ float bfhi(u32 v) { return __builtin_bit_cast(float, v & 0xffff0000u); }
__device__ __forceinline__ float rbf(float x) { return __bfloat162float(__float2bfloat16(x)); }
__device__ __forceinline__ u32 pk_bf(float lo, float hi) {
  const u32 a = __builtin_bit_cast(u16, __float2bfloat16(lo));
  const u32 b = __builtin_bit_cast(u16, __float2bfloat16(hi));
  return a | (b << 16);
}
template <int CTRL> __device__ __forceinline__ float dpp(float x) {
  return __builtin_bit_cast(float, __builtin_amdgcn_mov_dpp(__builtin_bit_cast(int, x), CTRL, 0xf, 0xf, true));
}
constexpr int XOR1 = 0xB1, XOR2 = 0x4E, XOR7 = 0x141, XOR8 = 0x128, BCAST = 0x150;

__device__ __forceinline__ float xrow_sum(float x) {
  auto s = __builtin_amdgcn_permlane16_swap(__float_as_uint(x), __float_as_uint(x), false, false);
  x = __uint_as_float(s[0]) + __uint_as_float(s[1]);
  auto t = __builtin_amdgcn_permlane32_swap(__float_as_uint(x), __float_as_uint(x), false, false);
  return __uint_as_float(t[0]) + __uint_as_float(t[1]);
}
__device__ __forceinline__ float xrow_max(float x) {
  auto s = __builtin_amdgcn_permlane16_swap(__float_as_uint(x), __float_as_uint(x), false, false);
  x = fmaxf(__uint_as_float(s[0]), __uint_as_float(s[1]));
  auto t = __builtin_amdgcn_permlane32_swap(__float_as_uint(x), __float_as_uint(x), false, false);
  return fmaxf(__uint_as_float(t[0]), __uint_as_float(t[1]));
}
__device__ __forceinline__ float row_sum(float x) {
  x += dpp<XOR1>(x); x += dpp<XOR2>(x); x += dpp<XOR7>(x); x += dpp<XOR8>(x); return x;
}
__device__ __forceinline__ float row_max(float x) {
  x = fmaxf(x, dpp<XOR1>(x)); x = fmaxf(x, dpp<XOR2>(x)); x = fmaxf(x, dpp<XOR7>(x)); x = fmaxf(x, dpp<XOR8>(x)); return x;
}
__device__ __forceinline__ float tr_reduce16(float (&x)[16], int c) {
#define TR_STEP(HALF, CTRL, BIT)                                                     \
  _Pragma("unroll") for (int h = 0; h < (HALF); ++h) {                               \
    const float lo_ = x[h], hi_ = x[h + (HALF)];                                     \
    const bool up_ = (c & (BIT)) != 0;                                               \
    const float send_ = up_ ? lo_ : hi_, keep_ = up_ ? hi_ : lo_;                    \
    x[h] = keep_ + dpp<CTRL>(send_);                                                 \
  }
  TR_STEP(8, XOR8, 8) TR_STEP(4, XOR7, 4) TR_STEP(2, XOR2, 2) TR_STEP(1, XOR1, 1)
#undef TR_STEP
  return x[0];
}

#define SCHED_FENCE() __builtin_amdgcn_sched_barrier(0)

__device__ __forceinline__ u32x4 ld_nt(const u16* ub, u32 off) {
  return __builtin_nontemporal_load(reinterpret_cast<const u32x4*>(reinterpret_cast<const char*>(ub) + off));
}
__device__ __forceinline__ u32x4 ld16(const u16* ub, u32 off) {
  return *reinterpret_cast<const u32x4*>(reinterpret_cast<const char*>(ub) + off);
}
__device__ __forceinline__ u32x2 ld8(const u16* ub, u32 off) {
  return *reinterpret_cast<const u32x2*>(reinterpret_cast<const char*>(ub) + off);
}
__device__ __forceinline__ u32 ld2(const u16* ub, u32 off) {
  return *reinterpret_cast<const u16*>(reinterpret_cast<const char*>(ub) + off);
}

constexpr int L_QP = 0;
constexpr int L_F1 = L_QP + NWAVE * DH;
constexpr int L_OP = L_F1 + NWAVE * 16;
constexpr int L_ML = L_OP + NWAVE * DH;
constexpr int L_RED = L_ML + NWAVE * 2;
constexpr int L_END = L_RED + NWAVE * DM;

__global__ void __launch_bounds__(512, 2)
k_main(const u16* __restrict__ x, const u16* __restrict__ kc, const u16* __restrict__ vc, const u16* __restrict__ wq,
       const u16* __restrict__ wo, const u16* __restrict__ w1, const u16* __restrict__ w2, float* __restrict__ ws) {
  __shared__ __attribute__((aligned(16))) float lds[L_END];
  const int bid = blockIdx.x, xcd = bid & 7, idx = bid >> 3;
  const int h = xcd * 4 + (idx >> 3), b = (idx >> 2) & 1, s = idx & 3;
  const int tid = threadIdx.x, lane = tid & 63, wave = __builtin_amdgcn_readfirstlane(tid >> 6);
  const int r = lane >> 4, c = lane & 15;
  const bool ffn_on = idx < 27;
  const int fg = xcd * 27 + (ffn_on ? idx : 26);
  u32x2 xq[5];
#pragma unroll
  for (int j = 0; j < 5; ++j) xq[j] = ld8(x + b * DM + wave * 80 + 4 * j, r * 40);
  SCHED_FENCE();
  u32x4 Wq[20], KV[64], Wo[20];
  const size_t kvbase = ((size_t)(b * NH + h) * SK + s * TOKWG + ((wave >> 2) * 128 + (wave & 3)) * 4) * DH;
  const u16* kp = kc + kvbase;
  const u16* vp = vc + kvbase;
  const u32 loff = lane * 16;
#define LDKV(I) ld_nt(((I) < 32 ? kp + (I) * 16 * DH : vp + ((I) - 32) * 16 * DH), loff)
  {
    const u32 off = (u32)(r * 20) * (NH * DH * 2) + 16 * c;
#pragma unroll
    for (int j = 0; j < 20; ++j) {
      Wq[j] = ld16(wq + (size_t)(wave * 80 + j) * (NH * DH) + h * DH, off);
      SCHED_FENCE();
      KV[j] = LDKV(j);
      SCHED_FENCE();
    }
  }
#pragma unroll
  for (int i = 20; i < 24; ++i) { KV[i] = LDKV(i); SCHED_FENCE(); }
  const u16* wop = wo + (size_t)(h * DH + wave * 16) * DM;
  const u32 wooff = (u32)r * (4 * DM * 2) + 16 * c;
  constexpr int PRE = 5;
#define ISSUE_T(T)                                                                                  \
  do {                                                                                              \
    if ((T) >= 0 && (T) < 20) {                                                                     \
      SCHED_FENCE();                                                                                \
      Wo[T] = ld16(wop + (2 * (((T) >> 1) / 5) + ((T) & 1)) * DM + (((T) >> 1) % 5) * 128, wooff);  \
      SCHED_FENCE();                                                                                \
      _Pragma("unroll") for (int t_ = 0; t_ < 2; ++t_) {                                            \
        const int kv_ = 24 + 2 * (T) + t_;                                                          \
        KV[kv_] = LDKV(kv_);                                                                        \
        SCHED_FENCE();                                                                              \
      }                                                                                             \
    }                                                                                               \
  } while (0)
#define PERM_Q(Q)                                                                                   \
  do {                                                                                              \
    _Pragma("unroll") for (int k_ = 0; k_ < 4; ++k_) {                                              \
      const u32 a_ = Wo[2 * (Q)][k_], b_ = Wo[2 * (Q) + 1][k_];                                     \
      Wo[2 * (Q)][k_] = __builtin_amdgcn_perm(b_, a_, 0x05040100u);                                 \
      Wo[2 * (Q) + 1][k_] = __builtin_amdgcn_perm(b_, a_, 0x07060302u);                             \
    }                                                                                               \
    asm volatile("" : "+v"(Wo[2 * (Q)]), "+v"(Wo[2 * (Q) + 1]));                                     \
  } while (0)
#define AFTER_KV(I)                                                                                 \
  do {                                                                                              \
    if ((I) % 3 == 2) ISSUE_T(PRE + (I) / 3);                                                       \
    if ((I) >= 26 && ((I) - 26) % 4 == 0) PERM_Q(((I) - 26) / 4);                                   \
  } while (0)

  {
    float qa[8];
#pragma unroll
    for (int k = 0; k < 8; ++k) qa[k] = 0.f;
#pragma unroll
    for (int j = 0; j < 20; ++j) {
      const u32 xw = xq[j >> 2][(j >> 1) & 1];
      const float xv = (j & 1) ? bfhi(xw) : bflo(xw);
#pragma unroll
      for (int k = 0; k < 4; ++k) {
        qa[2 * k] = fmaf(xv, bflo(Wq[j][k]), qa[2 * k]);
        qa[2 * k + 1] = fmaf(xv, bfhi(Wq[j][k]), qa[2 * k + 1]);
      }
    }
#pragma unroll
    for (int k = 0; k < 8; ++k) qa[k] = xrow_sum(qa[k]);
    if (r == 0) {
      *reinterpret_cast<f32x4*>(&lds[L_QP + wave * DH + 8 * c]) = f32x4{qa[0], qa[1], qa[2], qa[3]};
      *reinterpret_cast<f32x4*>(&lds[L_QP + wave * DH + 8 * c + 4]) = f32x4{qa[4], qa[5], qa[6], qa[7]};
    }
  }
  SCHED_FENCE();
  const u32x4 w1a = ld16(w1 + (size_t)(wave * 80) * FF + fg * 8, (u32)lane * (FF * 2));
  const u32x4 w1b = ld16(w1 + (size_t)(wave * 80 + 64) * FF + fg * 8, (u32)c * (FF * 2));
  const u32 xa0 = ld2(x + wave * 80, 2 * lane), xa1 = ld2(x + DM + wave * 80, 2 * lane);
  const u32 xb0 = ld2(x + wave * 80 + 64, 2 * c), xb1 = ld2(x + DM + wave * 80 + 64, 2 * c);
  SCHED_FENCE();
#pragma unroll
  for (int T = 0; T < PRE; ++T) ISSUE_T(T);
  __syncthreads();
  u32 qpk[4];
  {
    f32x4 q0 = {0.f, 0.f, 0.f, 0.f}, q1 = {0.f, 0.f, 0.f, 0.f};
#pragma unroll
    for (int w = 0; w < NWAVE; ++w) {
      q0 += *reinterpret_cast<const f32x4*>(&lds[L_QP + w * DH + 8 * c]);
      q1 += *reinterpret_cast<const f32x4*>(&lds[L_QP + w * DH + 8 * c + 4]);
    }
    qpk[0] = pk_bf(q0[0], q0[1]); qpk[1] = pk_bf(q0[2], q0[3]);
    qpk[2] = pk_bf(q1[0], q1[1]); qpk[3] = pk_bf(q1[2], q1[3]);
  }

  float sc[2];
  u32x2 w2r[8];
#pragma unroll
  for (int g = 0; g < 2; ++g) {
    float xx[16];
#pragma unroll
    for (int i = 0; i < 16; ++i) {
      const u32x4 kk = KV[g * 16 + i];
      float d = dot2(kk[0], qpk[0], 0.f);
      d = dot2(kk[1], qpk[1], d);
      d = dot2(kk[2], qpk[2], d);
      xx[i] = dot2(kk[3], qpk[3], d);
      AFTER_KV(g * 16 + i);
    }
    const float t = tr_reduce16(xx, c);
    sc[g] = rbf(rbf(t) * SCALE);
    if (g == 0) {
      SCHED_FENCE();
      float fa[16];
      const float a0 = bflo(xa0), a1 = bflo(xa1);
      const float b0 = lane < 16 ? bflo(xb0) : 0.f, b1 = lane < 16 ? bflo(xb1) : 0.f;
#pragma unroll
      for (int k = 0; k < 4; ++k) {
        fa[2 * k] = fmaf(a0, bflo(w1a[k]), b0 * bflo(w1b[k]));
        fa[2 * k + 1] = fmaf(a0, bfhi(w1a[k]), b0 * bfhi(w1b[k]));
        fa[8 + 2 * k] = fmaf(a1, bflo(w1a[k]), b1 * bflo(w1b[k]));
        fa[8 + 2 * k + 1] = fmaf(a1, bfhi(w1a[k]), b1 * bfhi(w1b[k]));
      }
      float ft = tr_reduce16(fa, c);
      ft = xrow_sum(ft);
      if (r == 0) lds[L_F1 + wave * 16 + c] = ft;
      SCHED_FENCE();
      const u32 col = (tid < 160 ? tid : 0) * 8;
#pragma unroll
      for (int j = 0; j < 8; ++j) w2r[j] = ld8(w2 + (size_t)(fg * 8 + j) * DM, col);
      SCHED_FENCE();
    }
  }
  {
    const float mw = xrow_max(row_max(fmaxf(sc[0], sc[1])));
    if (lane == 0) lds[L_ML + wave] = mw;
  }
  __syncthreads();
  float m2;
  {
    const f32x4 t0 = *reinterpret_cast<const f32x4*>(&lds[L_ML]), t1 = *reinterpret_cast<const f32x4*>(&lds[L_ML + 4]);
    m2 = fmaxf(fmaxf(fmaxf(t0[0], t0[1]), fmaxf(t0[2], t0[3])), fmaxf(fmaxf(t1[0], t1[1]), fmaxf(t1[2], t1[3]))) * LOG2E;
  }
  float p[2];
  p[0] = __builtin_amdgcn_exp2f(fmaf(sc[0], LOG2E, -m2));
  p[1] = __builtin_amdgcn_exp2f(fmaf(sc[1], LOG2E, -m2));
  const float lsum = xrow_sum(row_sum(p[0] + p[1]));
  if (lane == 0) lds[L_ML + 8 + wave] = lsum;

  {
    float hval;
    float t = 0.f;
#pragma unroll
    for (int w = 0; w < NWAVE; ++w) t += lds[L_F1 + w * 16 + c];
    t = rbf(t);
    hval = rbf(t / (1.f + __expf(-t)));
    float f0[4] = {0.f, 0.f, 0.f, 0.f}, f1[4] = {0.f, 0.f, 0.f, 0.f};
#define FFN2_STEP(J)                                                                  \
    {                                                                                 \
      const float h0 = dpp<BCAST + (J)>(hval), h1 = dpp<BCAST + 8 + (J)>(hval);       \
      const float w0 = bflo(w2r[J][0]), w1_ = bfhi(w2r[J][0]), w2_ = bflo(w2r[J][1]), w3 = bfhi(w2r[J][1]); \
      f0[0] = fmaf(h0, w0, f0[0]); f0[1] = fmaf(h0, w1_, f0[1]); f0[2] = fmaf(h0, w2_, f0[2]); f0[3] = fmaf(h0, w3, f0[3]); \
      f1[0] = fmaf(h1, w0, f1[0]); f1[1] = fmaf(h1, w1_, f1[1]); f1[2] = fmaf(h1, w2_, f1[2]); f1[3] = fmaf(h1, w3, f1[3]); \
    }
    FFN2_STEP(0) FFN2_STEP(1) FFN2_STEP(2) FFN2_STEP(3) FFN2_STEP(4) FFN2_STEP(5) FFN2_STEP(6) FFN2_STEP(7)
#undef FFN2_STEP
    asm volatile("" : "+v"(f0[0]), "+v"(f0[1]), "+v"(f0[2]), "+v"(f0[3]), "+v"(f1[0]), "+v"(f1[1]), "+v"(f1[2]), "+v"(f1[3]));
    if (ffn_on && tid < 160) {
      float* fp = ws + WS_F + (size_t)fg * NB * DM + tid * 4;
      *reinterpret_cast<f32x4*>(fp) = f32x4{f0[0], f0[1], f0[2], f0[3]};
      *reinterpret_cast<f32x4*>(fp + DM) = f32x4{f1[0], f1[1], f1[2], f1[3]};
    }
  }
  SCHED_FENCE();

  float acc[8];
#pragma unroll
  for (int k = 0; k < 8; ++k) acc[k] = 0.f;
#define PV_STEP(I)                                                                    \
  {                                                                                   \
    const float pb = dpp<BCAST + ((I) & 15)>(p[(I) >> 4]);                            \
    const u32x4 vv = KV[32 + (I)];                                                    \
    _Pragma("unroll") for (int k = 0; k < 4; ++k) {                                   \
      acc[2 * k] = fmaf(pb, bflo(vv[k]), acc[2 * k]);                                 \
      acc[2 * k + 1] = fmaf(pb, bfhi(vv[k]), acc[2 * k + 1]);                         \
    }                                                                                 \
    AFTER_KV(32 + (I));                                                               \
  }
  PV_STEP(0) PV_STEP(1) PV_STEP(2) PV_STEP(3) PV_STEP(4) PV_STEP(5) PV_STEP(6) PV_STEP(7)
  PV_STEP(8) PV_STEP(9) PV_STEP(10) PV_STEP(11) PV_STEP(12) PV_STEP(13) PV_STEP(14) PV_STEP(15)
  PV_STEP(16) PV_STEP(17) PV_STEP(18) PV_STEP(19) PV_STEP(20) PV_STEP(21) PV_STEP(22) PV_STEP(23)
  PV_STEP(24) PV_STEP(25) PV_STEP(26) PV_STEP(27)
  float y[40];
#pragma unroll
  for (int v = 0; v < 40; v += 8) {
#pragma unroll
    for (int k = 0; k < 8; ++k) y[v + k] = 0.f;
    asm volatile("" : "+v"(y[v]), "+v"(y[v + 1]), "+v"(y[v + 2]), "+v"(y[v + 3]), "+v"(y[v + 4]), "+v"(y[v + 5]), "+v"(y[v + 6]), "+v"(y[v + 7]));
  }
  SCHED_FENCE();
  PV_STEP(28) PV_STEP(29) PV_STEP(30) PV_STEP(31)
#undef PV_STEP
  {
    float z4[4], z2[2];
#pragma unroll
    for (int k = 0; k < 4; ++k) {
      auto t = __builtin_amdgcn_permlane32_swap(__float_as_uint(acc[k]), __float_as_uint(acc[k + 4]), false, false);
      z4[k] = __uint_as_float(t[0]) + __uint_as_float(t[1]);
    }
#pragma unroll
    for (int k = 0; k < 2; ++k) {
      auto t = __builtin_amdgcn_permlane16_swap(__float_as_uint(z4[k]), __float_as_uint(z4[k + 2]), false, false);
      z2[k] = __uint_as_float(t[0]) + __uint_as_float(t[1]);
    }
    *reinterpret_cast<f32x2*>(&lds[L_OP + wave * DH + 8 * c + 2 * r]) = f32x2{z2[0], z2[1]};
  }
  __syncthreads();
  {
    f32x4 o4 = {0.f, 0.f, 0.f, 0.f};
#pragma unroll
    for (int w = 0; w < 8; ++w) o4 += *reinterpret_cast<const f32x4*>(&lds[L_OP + w * DH + (wave * 4 + r) * 4]);
    const u32 o01 = pk_bf(o4[0], o4[1]), o23 = pk_bf(o4[2], o4[3]);
#pragma unroll
    for (int seg = 0; seg < 5; ++seg)
#pragma unroll
      for (int k = 0; k < 4; ++k) {
        y[(2 * k) * 5 + seg] = dot2(Wo[2 * (5 + seg)][k], o23, dot2(Wo[2 * seg][k], o01, y[(2 * k) * 5 + seg]));
        y[(2 * k + 1) * 5 + seg] = dot2(Wo[2 * (5 + seg) + 1][k], o23, dot2(Wo[2 * seg + 1][k], o01, y[(2 * k + 1) * 5 + seg]));
      }
    float z[20];
#pragma unroll
    for (int v = 0; v < 20; ++v) {
      auto t = __builtin_amdgcn_permlane32_swap(__float_as_uint(y[v]), __float_as_uint(y[v + 20]), false, false);
      z[v] = __uint_as_float(t[0]) + __uint_as_float(t[1]);
    }
    float u[10];
#pragma unroll
    for (int v = 0; v < 10; ++v) {
      auto t = __builtin_amdgcn_permlane16_swap(__float_as_uint(z[v]), __float_as_uint(z[v + 10]), false, false);
      u[v] = __uint_as_float(t[0]) + __uint_as_float(t[1]);
    }
#pragma unroll
    for (int seg = 0; seg < 5; ++seg)
      *reinterpret_cast<f32x2*>(&lds[L_RED + wave * DM + seg * 128 + 8 * c + 2 * r]) = f32x2{u[seg], u[5 + seg]};
  }
  __syncthreads();
  if (tid < 160) {
    f32x4 t = {0.f, 0.f, 0.f, 0.f};
#pragma unroll
    for (int w = 0; w < NWAVE; ++w) t += *reinterpret_cast<const f32x4*>(&lds[L_RED + w * DM + tid * 4]);
    *reinterpret_cast<f32x4*>(ws + WS_P + (size_t)((b * NH + h) * NSPLIT + s) * DM + tid * 4) = t;
  }
  if (tid == 256) {
    const f32x4 t0 = *reinterpret_cast<const f32x4*>(&lds[L_ML + 8]), t1 = *reinterpret_cast<const f32x4*>(&lds[L_ML + 12]);
    const float L = ((t0[0] + t0[1]) + (t0[2] + t0[3])) + ((t1[0] + t1[1]) + (t1[2] + t1[3]));
    *reinterpret_cast<f32x2*>(ws + WS_ML + (size_t)((b * NH + h) * NSPLIT + s) * 2) = f32x2{m2, L};
  }
}

__global__ void __launch_bounds__(256)
k_fin(const u16* __restrict__ x, const u16* __restrict__ gw, const float* __restrict__ ws, u16* __restrict__ out) {
  __shared__ __attribute__((aligned(16))) float red[4][2][32];
  const int b = blockIdx.x / 20, cb = blockIdx.x % 20;
  const int tid = threadIdx.x, lane = tid & 63, wave = tid >> 6;
  const int c4 = tid & 7, rg = tid >> 3;
  const int col = cb * 32 + c4 * 4;
  f32x4 pr[4];
#pragma unroll
  for (int s = 0; s < 4; ++s)
    pr[s] = *reinterpret_cast<const f32x4*>(ws + WS_P + (size_t)((b * NH + rg) * NSPLIT + s) * DM + col);
  const f32x4 ml0 = *reinterpret_cast<const f32x4*>(ws + WS_ML + (size_t)(b * NH + rg) * NSPLIT * 2);
  const f32x4 ml1 = *reinterpret_cast<const f32x4*>(ws + WS_ML + (size_t)(b * NH + rg) * NSPLIT * 2 + 4);
  f32x4 fr[7];
#pragma unroll
  for (int j = 0; j < 7; ++j) {
    const int f = rg + 32 * j;
    const int fc = f < NFG ? f : NFG - 1;
    fr[j] = *reinterpret_cast<const f32x4*>(ws + WS_F + (size_t)(fc * NB + b) * DM + col);
  }
  u32 xg[5], gg[5];
#pragma unroll
  for (int j = 0; j < 5; ++j) {
    xg[j] = *reinterpret_cast<const u32*>(x + b * DM + 2 * (lane + 64 * j));
    gg[j] = *reinterpret_cast<const u32*>(gw + 2 * (lane + 64 * j));
  }
  float gs = 0.f;
#pragma unroll
  for (int j = 0; j < 5; ++j) gs += rbf(bflo(xg[j]) * bflo(gg[j])) + rbf(bfhi(xg[j]) * bfhi(gg[j]));
  gs = xrow_sum(row_sum(gs));
  gs = rbf(gs);
  const float g = rbf(1.f / (1.f + __expf(-gs)));
  const float M = fmaxf(fmaxf(ml0[0], ml0[2]), fmaxf(ml1[0], ml1[2]));
  const float e0 = __builtin_amdgcn_exp2f(ml0[0] - M), e1 = __builtin_amdgcn_exp2f(ml0[2] - M);
  const float e2 = __builtin_amdgcn_exp2f(ml1[0] - M), e3 = __builtin_amdgcn_exp2f(ml1[2] - M);
  const float L = e0 * ml0[1] + e1 * ml0[3] + e2 * ml1[1] + e3 * ml1[3];
  const float inv = 1.f / L;
  f32x4 a = (e0 * inv) * pr[0] + (e1 * inv) * pr[1] + (e2 * inv) * pr[2] + (e3 * inv) * pr[3];
  f32x4 f = fr[0] + fr[1] + fr[2] + fr[3] + fr[4] + fr[5];
  if (rg + 32 * 6 < NFG) f += fr[6];
#pragma unroll
  for (int k = 0; k < 4; ++k) {
    a[k] += dpp<XOR8>(a[k]); a[k] = xrow_sum(a[k]);
    f[k] += dpp<XOR8>(f[k]); f[k] = xrow_sum(f[k]);
  }
  if (lane < 8) {
    *reinterpret_cast<f32x4*>(&red[wave][0][c4 * 4]) = a;
    *reinterpret_cast<f32x4*>(&red[wave][1][c4 * 4]) = f;
  }
  __syncthreads();
  if (tid < 32) {
    const float av = rbf(red[0][0][tid] + red[1][0][tid] + red[2][0][tid] + red[3][0][tid]);
    const float fv = rbf(red[0][1][tid] + red[1][1][tid] + red[2][1][tid] + red[3][1][tid]);
    const float t1 = rbf(g * av), t2 = rbf(rbf(1.f - g) * fv);
    out[b * DM + cb * 32 + tid] = __builtin_bit_cast(u16, __float2bfloat16(t1 + t2));
  }
}

}

extern "C" void kernel_launch(void* const* d_in, const int* in_sizes, int n_in, void* d_out, int out_size, void* d_ws,
                              size_t ws_size, hipStream_t stream) {
  const u16* x = (const u16*)d_in[0];
  const u16* kc = (const u16*)d_in[1];
  const u16* vc = (const u16*)d_in[2];
  const u16* wq = (const u16*)d_in[3];
  const u16* wo = (const u16*)d_in[4];
  const u16* w1 = (const u16*)d_in[6];
  const u16* w2 = (const u16*)d_in[7];
  const u16* gw = (const u16*)d_in[8];
  float* ws = (float*)d_ws;
  k_main<<<dim3(NB * NH * NSPLIT), dim3(512), 0, stream>>>(x, kc, vc, wq, wo, w1, w2, ws);
  k_fin<<<dim3(NB * 20), dim3(256), 0, stream>>>(x, gw, ws, (u16*)d_out);
}
```
